```python
import torch

def _softmax(x, axis=-1):
    return torch.softmax(x, dim=axis)
def _gelu(x):
    return torch.nn.functional.gelu(x, approximate="tanh")
def _silu(x):
    return torch.nn.functional.silu(x)
def _sigmoid(x):
    return torch.sigmoid(x)
def _layernorm(x, axis=-1, eps=1e-5):
    m = x.mean(dim=axis, keepdim=True)
    v = x.var(dim=axis, keepdim=True, unbiased=False)
    return (x - m) / torch.sqrt(v + eps)
def _scatter_add(tgt, idx, src, axis=0):
    return tgt.clone().index_add_(axis, idx, src)
def _segment_sum(src, seg, n):
    out = torch.zeros((n,) + src.shape[1:], dtype=src.dtype)
    return out.index_add_(0, seg, src)
def _pad(x, pw):
    flat = tuple(v for pair in reversed(pw) for v in pair)
    return torch.nn.functional.pad(x, flat)

kernel_name = 'concat_heads__gqa_block_cached__sliding_block_cached__decode'

def setup_inputs():

    _w = (lambda a: (lambda t: t.to(torch.bfloat16) if t.is_floating_point() else t)(torch.as_tensor(a)))
    def _A_setup():
        B, Sq, D = 2, 1, 512
        Hq, Hkv, Sk, Dh = 32, 8, 8192, 128
        assert Hq % Hkv == 0, (Hq, Hkv)
        return {
            "x": _w((lambda *s: torch.randn(*s, dtype=torch.bfloat16) * 0.8)(B, Sq, D)),
            "k_cache": _w((lambda *s: torch.randn(*s, dtype=torch.bfloat16) * 1.6)(B, Hkv, Sk, Dh)),
            "v_cache": _w((lambda *s: torch.randn(*s, dtype=torch.bfloat16) * 0.2)(B, Hkv, Sk, Dh)),
            "wq": _w((lambda *s: torch.randn(*s, dtype=torch.bfloat16) * 1.6)(D, Hq * Dh) * (D ** -0.5)),
            "wo": _w((lambda *s: torch.randn(*s, dtype=torch.bfloat16) * 0.2)(Hq * Dh, D) * ((Hq * Dh) ** -0.5)),
        }
    _A_in = _A_setup()

    def _B_setup():
        B, Sq, D = 2, 1, 512
        Hq, Hkv, Sk, Dh = 32, 8, 8192, 128
        assert Hq % Hkv == 0, (Hq, Hkv)
        return {
            "x": _w((lambda *s: torch.randn(*s, dtype=torch.bfloat16) * 0.8)(B, Sq, D)),
            "k_cache": _w((lambda *s: torch.randn(*s, dtype=torch.bfloat16) * 1.6)(B, Hkv, Sk, Dh)),
            "v_cache": _w((lambda *s: torch.randn(*s, dtype=torch.bfloat16) * 0.2)(B, Hkv, Sk, Dh)),
            "wq": _w((lambda *s: torch.randn(*s, dtype=torch.bfloat16) * 1.6)(D, Hq * Dh) * (D ** -0.5)),
            "wo": _w((lambda *s: torch.randn(*s, dtype=torch.bfloat16) * 0.2)(Hq * Dh, D) * ((Hq * Dh) ** -0.5)),
        }
    _B_in = _B_setup()

    return {
        "A_x": _A_in["x"],
        "A_k_cache": _A_in["k_cache"],
        "A_v_cache": _A_in["v_cache"],
        "A_wq": _A_in["wq"],
        "A_wo": _A_in["wo"],
        "B_x": _B_in["x"],
        "B_k_cache": _B_in["k_cache"],
        "B_v_cache": _B_in["v_cache"],
        "B_wq": _B_in["wq"],
        "B_wo": _B_in["wo"],
    }

def reference(A_x, A_k_cache, A_v_cache, A_wq, A_wo, B_x, B_k_cache, B_v_cache, B_wq, B_wo):
    def _A_ref(x, k_cache, v_cache, wq, wo):
        B, Sq, D = x.shape
        Hq, Dh = 32, 128
        Hkv = k_cache.shape[1]
        rep = Hq // Hkv
        q = (lambda x, *d: x.permute(*d))((x @ wq).reshape(B, Sq, Hq, Dh), 0, 2, 1, 3)
        k = (lambda x, n, axis: torch.repeat_interleave(x, n, dim=axis))(k_cache, rep, axis=1)
        v = (lambda x, n, axis: torch.repeat_interleave(x, n, dim=axis))(v_cache, rep, axis=1)
        scores = torch.einsum("bhqd,bhkd->bhqk", q, k) * (Dh ** -0.5)
        p = _softmax(scores, axis=-1)
        o = torch.einsum("bhqk,bhkd->bhqd", p, v)
        return (lambda x, *d: x.permute(*d))(o, 0, 2, 1, 3).reshape(B, Sq, Hq * Dh) @ wo

    def _B_ref(x, k_cache, v_cache, wq, wo):
        B, Sq, D = x.shape
        Hq, Dh = 32, 128
        Hkv, Sk = k_cache.shape[1], k_cache.shape[2]
        W = min(4096, max(256, Sk // 4))
        rep = Hq // Hkv
        q = (lambda x, *d: x.permute(*d))((x @ wq).reshape(B, Sq, Hq, Dh), 0, 2, 1, 3)
        k = (lambda x, n, axis: torch.repeat_interleave(x, n, dim=axis))(k_cache[:, :, Sk - W:, :], rep, axis=1)
        v = (lambda x, n, axis: torch.repeat_interleave(x, n, dim=axis))(v_cache[:, :, Sk - W:, :], rep, axis=1)
        scores = torch.einsum("bhqd,bhkd->bhqk", q, k) * (Dh ** -0.5)
        p = _softmax(scores, axis=-1)
        o = torch.einsum("bhqk,bhkd->bhqd", p, v)
        return (lambda x, *d: x.permute(*d))(o, 0, 2, 1, 3).reshape(B, Sq, Hq * Dh) @ wo

    a = _A_ref(A_x, A_k_cache, A_v_cache, A_wq, A_wo)
    b = _B_ref(B_x, B_k_cache, B_v_cache, B_wq, B_wo)
    return torch.concatenate([a, b], axis=-1)
```

```cpp
#include <hip/hip_runtime.h>
#include <hip/hip_bf16.h>
#include <stdint.h>
typedef unsigned short u16;
typedef unsigned int u32;
__device__ __forceinline__ float bf2f(u16 v) { return __uint_as_float(((u32)v) << 16); }
__device__ __forceinline__ u16 f2bf(float f) { u32 u = __float_as_uint(f); u += 0x7FFFu + ((u >> 16) & 1u); return (u16)(u >> 16); }
constexpr int D = 512, HQ = 32, HKV = 8, SK = 8192, DH = 128, W = 2048;
__global__ void k_q(const u16* xA, const u16* wqA, const u16* xB, const u16* wqB, float* q) {
  int br = blockIdx.y; int n = blockIdx.x * 256 + threadIdx.x;
  const u16* x = br ? xB : xA; const u16* w = br ? wqB : wqA;
  float a0 = 0, a1 = 0;
  for (int k = 0; k < D; ++k) { float wv = bf2f(w[(size_t)k * 4096 + n]); a0 += bf2f(x[k]) * wv; a1 += bf2f(x[D + k]) * wv; }
  q[(br * 2 + 0) * 4096 + n] = bf2f(f2bf(a0)); q[(br * 2 + 1) * 4096 + n] = bf2f(f2bf(a1));
}
__global__ void k_attn(const u16* kA, const u16* vA, const u16* kB, const u16* vB, const float* q, float* po, float* pml) {
  int unit = blockIdx.x;
  int hq = blockIdx.y, b = blockIdx.z; int br = unit >= 32; int s = br ? unit - 32 : unit;
  const u16* K = br ? kB : kA; const u16* V = br ? vB : vA;
  int key0 = br ? (SK - W + s * 256) : s * 256;
  int hk = hq / 4;
  __shared__ float sq[128]; __shared__ float sp[256]; __shared__ float red[256];
  int t = threadIdx.x;
  if (t < 128) sq[t] = q[(br * 2 + b) * 4096 + hq * 128 + t];
  __syncthreads();
  const u16* kr = K + ((size_t)(b * HKV + hk) * SK + key0 + t) * DH;
  float sc = 0; for (int d = 0; d < 128; ++d) sc += sq[d] * bf2f(kr[d]);
  sc *= 0.08838834764831845f;
  red[t] = sc; __syncthreads();
  for (int o = 128; o > 0; o >>= 1) { if (t < o) red[t] = fmaxf(red[t], red[t + o]); __syncthreads(); }
  float m = red[0]; __syncthreads();
  float p = __expf(sc - m); sp[t] = p; red[t] = p; __syncthreads();
  for (int o = 128; o > 0; o >>= 1) { if (t < o) red[t] += red[t + o]; __syncthreads(); }
  float l = red[0];
  int slot = ((b * HQ + hq) * 40 + unit);
  if (t < 128) { float a = 0; const u16* vr = V + ((size_t)(b * HKV + hk) * SK + key0) * DH + t; for (int j = 0; j < 256; ++j) a += sp[j] * bf2f(vr[(size_t)j * DH]); po[(size_t)slot * 128 + t] = a; }
  if (t == 0) { pml[slot * 2] = m; pml[slot * 2 + 1] = l; }
}
__global__ void k_comb(const float* po, const float* pml, float* o) {
  int hq = blockIdx.x, b = blockIdx.y, br = blockIdx.z, t = threadIdx.x;
  int u0 = br ? 32 : 0, u1 = br ? 40 : 32;
  float M = -INFINITY; for (int u = u0; u < u1; ++u) M = fmaxf(M, pml[((b * HQ + hq) * 40 + u) * 2]);
  float L = 0, a = 0;
  for (int u = u0; u < u1; ++u) { int slot = (b * HQ + hq) * 40 + u; float w = __expf(pml[slot * 2] - M); L += w * pml[slot * 2 + 1]; a += w * po[(size_t)slot * 128 + t]; }
  o[(br * 2 + b) * 4096 + hq * 128 + t] = bf2f(f2bf(a / L));
}
__global__ void k_out(const float* o, const u16* woA, const u16* woB, u16* out) {
  int br = blockIdx.y; int n = blockIdx.x * 64 + (threadIdx.x & 63); int kq = threadIdx.x >> 6;
  const u16* w = br ? woB : woA;
  float a0 = 0, a1 = 0;
  for (int k = kq * 256; k < kq * 256 + 256; ++k) { float wv = bf2f(w[(size_t)k * 512 + n]); a0 += o[(br * 2) * 4096 + k] * wv; a1 += o[(br * 2 + 1) * 4096 + k] * wv; }
  __shared__ float r0[1024], r1[1024];
  r0[threadIdx.x] = a0; r1[threadIdx.x] = a1; __syncthreads();
  if (threadIdx.x < 64) { float s0 = 0, s1 = 0; for (int i = 0; i < 16; ++i) { s0 += r0[i * 64 + threadIdx.x]; s1 += r1[i * 64 + threadIdx.x]; }
    out[0 * 1024 + br * 512 + n] = f2bf(s0); out[1 * 1024 + br * 512 + n] = f2bf(s1); }
}
extern "C" void kernel_launch(void* const* d_in, const int* in_sizes, int n_in, void* d_out, int out_size, void* d_ws, size_t ws_size, hipStream_t stream) {
  const u16 *xA = (const u16*)d_in[0], *kA = (const u16*)d_in[1], *vA = (const u16*)d_in[2], *wqA = (const u16*)d_in[3], *woA = (const u16*)d_in[4];
  const u16 *xB = (const u16*)d_in[5], *kB = (const u16*)d_in[6], *vB = (const u16*)d_in[7], *wqB = (const u16*)d_in[8], *woB = (const u16*)d_in[9];
  float* q = (float*)d_ws; float* o = q + 4 * 4096; float* pml = o + 4 * 4096; float* po = pml + 2 * 32 * 40 * 2;
  k_q<<<dim3(16, 2), 256, 0, stream>>>(xA, wqA, xB, wqB, q);
  k_attn<<<dim3(40, 32, 2), 256, 0, stream>>>(kA, vA, kB, vB, q, po, pml);
  k_comb<<<dim3(32, 2, 2), 128, 0, stream>>>(po, pml, o);
  k_out<<<dim3(8, 2), 1024, 0, stream>>>(o, woA, woB, (u16*)d_out);
}
```

```cpp
#include <hip/hip_runtime.h>
#include <stdint.h>

typedef unsigned short u16;
typedef unsigned int u32;
typedef unsigned long long u64;
typedef u32 u32x2 __attribute__((ext_vector_type(2)));
typedef u32 u32x4 __attribute__((ext_vector_type(4)));
typedef float f32x2 __attribute__((ext_vector_type(2)));
typedef float f32x4 __attribute__((ext_vector_type(4)));
typedef short s16x4 __attribute__((ext_vector_type(4)));
typedef __bf16 bf16x2 __attribute__((ext_vector_type(2)));
typedef __bf16 bf16x8 __attribute__((ext_vector_type(8)));
#define GAS __attribute__((address_space(1)))
#define LAS __attribute__((address_space(3)))

#define WIN 6
#define KV_NT 0
#define DIAG 0

constexpr int SK = 8192, WND = 2048;
constexpr int NWAVE = 11, NTHR = NWAVE * 64;
constexpr u32 SENT = 0xFFFFFFFFu;
constexpr int AUX_SC1 = 16;
constexpr int WS_EPOCH = 0;
constexpr int WS_QG = 4096;
constexpr int R1_STRIDE = 640;
constexpr int WS_R1 = WS_QG + 4 * 4096 * 8;
constexpr int WS_R2 = WS_R1 + 2048 * R1_STRIDE;
constexpr int WS_END = WS_R2 + 256 * 1024;
constexpr int WS_DIAG = 4 << 20;
constexpr u32 SPIN_CAP = 1u << 18;
constexpr int L_Q = 0;
constexpr int QSTR = 272;
constexpr int L_TAG = L_Q + 8 * QSTR;
constexpr int L_M = L_TAG + 16;
constexpr int L_L = L_M + 160;
constexpr int L_O = L_L + 160;
constexpr int OW = 4 * 132 * 4;
constexpr int L_O2 = L_O + 10 * OW;
constexpr int L_QR = L_O2;
constexpr int L_END = L_O2 + 8 * 1024;
static_assert(L_O % 16 == 0 && L_O2 % 16 == 0, "align");
constexpr int LDS_BYTES = 84 * 1024;

struct Params {
  const u16* x[2]; const u16* k[2]; const u16* v[2]; const u16* wq[2]; const u16* wo[2];
  u16* out; unsigned char* ws;
};

__device__ __forceinline__ u32x4 ldg_stream(const GAS unsigned char* p) {
  return *(const GAS u32x4*)p;
}
__device__ __forceinline__ f32x4 mfma32(u32x4 a, u32x4 b, f32x4 c) {
  return __builtin_amdgcn_mfma_f32_16x16x32_bf16(__builtin_bit_cast(bf16x8, a), __builtin_bit_cast(bf16x8, b), c, 0, 0, 0);
}
__device__ __forceinline__ f32x4 mfma16(u32x2 a, u32x2 b, f32x4 c) {
  return __builtin_amdgcn_mfma_f32_16x16x16bf16_1k(__builtin_bit_cast(s16x4, a), __builtin_bit_cast(s16x4, b), c, 0, 0, 0);
}
__device__ __forceinline__ u32 pk_lo(u32 a, u32 b) { return __builtin_amdgcn_perm(b, a, 0x05040100u); }
__device__ __forceinline__ u32 pk_hi(u32 a, u32 b) { return __builtin_amdgcn_perm(b, a, 0x07060302u); }
__device__ __forceinline__ u32 cvt_pk(float lo, float hi) {
  f32x2 f = {lo, hi};
  return __builtin_bit_cast(u32, __builtin_convertvector(f, bf16x2));
}
__device__ __forceinline__ float bf_lo(u32 v) { return __uint_as_float(v << 16); }
__device__ __forceinline__ float bf_hi(u32 v) { return __uint_as_float(v & 0xffff0000u); }
template <int CTRL> __device__ __forceinline__ float dpp(float x) {
  return __builtin_bit_cast(float, __builtin_amdgcn_mov_dpp(__builtin_bit_cast(int, x), CTRL, 0xf, 0xf, true));
}
constexpr int DPP_XOR1 = 0xB1, DPP_XOR2 = 0x4E, DPP_ROR2 = 0x122, DPP_ROR4 = 0x124, DPP_ROR8 = 0x128;
__device__ __forceinline__ float xrow_max(float x) {
  auto s = __builtin_amdgcn_permlane16_swap(__float_as_uint(x), __float_as_uint(x), false, false);
  x = fmaxf(__uint_as_float(s[0]), __uint_as_float(s[1]));
  auto t = __builtin_amdgcn_permlane32_swap(__float_as_uint(x), __float_as_uint(x), false, false);
  return fmaxf(__uint_as_float(t[0]), __uint_as_float(t[1]));
}
__device__ __forceinline__ float xrow_sum(float x) {
  auto s = __builtin_amdgcn_permlane16_swap(__float_as_uint(x), __float_as_uint(x), false, false);
  x = __uint_as_float(s[0]) + __uint_as_float(s[1]);
  auto t = __builtin_amdgcn_permlane32_swap(__float_as_uint(x), __float_as_uint(x), false, false);
  return __uint_as_float(t[0]) + __uint_as_float(t[1]);
}
__device__ __forceinline__ u32 clean_bits(float f) { u32 u = __float_as_uint(f); return u < 0xFFFFFFFEu ? u : 0xFFFFFFFEu; }
__device__ __forceinline__ u32 umax3(u32 a, u32 b, u32 c) { return max(max(a, b), c); }
#define SCHED() __builtin_amdgcn_sched_barrier(0)
#define BARRIER() do { asm volatile("s_waitcnt lgkmcnt(0)" ::: "memory"); __builtin_amdgcn_s_barrier(); asm volatile("" ::: "memory"); } while (0)
#define STAMP(i) do {} while (0)

__global__ void __launch_bounds__(NTHR, 3) fused_decode(Params p) {
  extern __shared__ __attribute__((aligned(16))) unsigned char smem[];
  LAS unsigned char* L = (LAS unsigned char*)smem;
  const int tid = threadIdx.x, lane = tid & 63;
  const int wave = __builtin_amdgcn_readfirstlane(tid >> 6);
  const int g = lane >> 4, c = lane & 15;
  const int h = blockIdx.x & 7, j = blockIdx.x >> 3;
  const int b = j >> 4, s = j & 15;
  const int wbr = j >> 4, whh = (j >> 2) & 3, wnb = j & 3;
  const __amdgpu_buffer_rsrc_t rs = __builtin_amdgcn_make_buffer_rsrc((void*)p.ws, 0, WS_END, 0x00020000);
  STAMP(0);

  if (wave == 0) {
    const u64 E = __hip_atomic_load((const GAS u64*)(p.ws + WS_EPOCH), __ATOMIC_RELAXED, __HIP_MEMORY_SCOPE_AGENT);
    const int qbr = j >> 4, qc0 = 512 * h + 32 * (j & 15);
    const GAS unsigned char* wq = (const GAS unsigned char*)p.wq[qbr] + (size_t)qc0 * 2 + (size_t)(8 * (lane >> 2)) * 8192 + (lane & 3) * 16;
    const GAS unsigned char* xp = (const GAS unsigned char*)p.x[qbr] + (c & 1) * 1024 + (4 * g + (c >> 2)) * 16;
    u32x4 xa[4], w[32];
#pragma unroll
    for (int i = 0; i < 4; ++i) xa[i] = *(const GAS u32x4*)(xp + i * 256);
#pragma unroll
    for (int i = 0; i < 24; ++i) w[i] = ldg_stream(wq + (size_t)((i >> 3) * 128 + (i & 7)) * 8192);
    SCHED();
    {
      const u32x4 sv = {SENT, SENT, SENT, SENT};
#pragma unroll
      for (int i = 0; i < 4; ++i) {
        const int rec = 2 * i + (lane >> 5);
        const int off = WS_R1 + ((((rec >> 2) * 32 + 4 * h + (rec & 3)) * 2 + b) * 16 + s) * R1_STRIDE + (lane & 31) * 16;
        __builtin_amdgcn_raw_buffer_store_b128(sv, rs, off, 0, AUX_SC1);
      }
      if (lane < 8) {
        const int off = WS_R1 + ((((lane >> 2) * 32 + 4 * h + (lane & 3)) * 2 + b) * 16 + s) * R1_STRIDE + 512;
        __builtin_amdgcn_raw_buffer_store_b128(sv, rs, off, 0, AUX_SC1);
      }
      __builtin_amdgcn_raw_buffer_store_b128(sv, rs, WS_R2 + ((wbr * 4 + wnb) * 32 + 4 * h + whh) * 1024 + lane * 16, 0, AUX_SC1);
    }
    SCHED();
    STAMP(1);
    f32x4 acc[8];
#pragma unroll
    for (int e = 0; e < 8; ++e) acc[e] = f32x4{0.f, 0.f, 0.f, 0.f};
#pragma unroll
    for (int gi = 0; gi < 4; ++gi) {
#pragma unroll
      for (int d = 0; d < 4; ++d) {
        u32x4 blo, bhi;
#pragma unroll
        for (int t = 0; t < 4; ++t) { blo[t] = pk_lo(w[gi * 8 + 2 * t][d], w[gi * 8 + 2 * t + 1][d]); bhi[t] = pk_hi(w[gi * 8 + 2 * t][d], w[gi * 8 + 2 * t + 1][d]); }
        acc[2 * d] = mfma32(xa[gi], blo, acc[2 * d]);
        acc[2 * d + 1] = mfma32(xa[gi], bhi, acc[2 * d + 1]);
      }
      if (gi == 0) {
        SCHED();
#pragma unroll
        for (int i = 24; i < 32; ++i) w[i] = ldg_stream(wq + (size_t)((i >> 3) * 128 + (i & 7)) * 8192);
        SCHED();
      }
    }
    LAS float* qr = (LAS float*)(L + L_QR);
    if (g == (c >> 2)) {
#pragma unroll
      for (int e = 0; e < 8; ++e) *(LAS f32x2*)(qr + (g * 32 + 8 * (c & 3) + e) * 2) = f32x2{acc[e][0], acc[e][1]};
    }
    asm volatile("s_waitcnt vmcnt(0) lgkmcnt(0)" ::: "memory");
    u64 z = E + 0x9E3779B97F4A7C15ull;
    z = (z ^ (z >> 30)) * 0xBF58476D1CE4E5B9ull; z = (z ^ (z >> 27)) * 0x94D049BB133111EBull; z ^= z >> 31;
    const u32 tlo = (u32)z & 0xFFFF0000u, thi = (u32)(z >> 32);
    {
      const int col = lane & 31, bb = lane >> 5;
      const float qv = (qr[(0 * 32 + col) * 2 + bb] + qr[(1 * 32 + col) * 2 + bb]) + (qr[(2 * 32 + col) * 2 + bb] + qr[(3 * 32 + col) * 2 + bb]);
      const u32 qb = cvt_pk(qv, qv) & 0xFFFFu;
      __builtin_amdgcn_raw_buffer_store_b64(u32x2{qb | tlo, thi}, rs, WS_QG + ((qbr * 2 + bb) * 4096 + qc0 + col) * 8, 0, AUX_SC1);
    }
    STAMP(2);
    if (lane == 0) *(LAS u64*)(L + L_TAG) = z;
    {
      const int rbr = blockIdx.x >> 7, rnb = (blockIdx.x >> 5) & 3;
      const int pj = rbr * 16 + (lane & 3) * 4 + rnb, ph = (lane >> 2) & 7;
      const int roff = WS_QG + (((pj >> 4) * 2) * 4096 + 512 * ph + 32 * (pj & 15)) * 8;
      u32x4 gq[8];
      for (u32 spin = 0;; ++spin) {
        const u32x2 rdy = __builtin_amdgcn_raw_buffer_load_b64(rs, roff, 0, AUX_SC1);
#pragma unroll
        for (int i = 0; i < 8; ++i) gq[i] = __builtin_amdgcn_raw_buffer_load_b128(rs, WS_QG + (((i >> 2) * 2 + b) * 4096 + 512 * h + (i & 3) * 128 + lane * 2) * 8, 0, AUX_SC1);
        u32 bad = ((rdy[0] ^ tlo) & 0xFFFF0000u) | (rdy[1] ^ thi);
#pragma unroll
        for (int i = 0; i < 8; ++i) bad |= (((gq[i][0] ^ tlo) | (gq[i][2] ^ tlo)) & 0xFFFF0000u) | (gq[i][1] ^ thi) | (gq[i][3] ^ thi);
        if (__builtin_amdgcn_ballot_w64(bad != 0) == 0 || spin >= SPIN_CAP) break;
        __builtin_amdgcn_s_sleep(2);
      }
#pragma unroll
      for (int i = 0; i < 8; ++i) *(LAS u32*)(L + L_Q + i * QSTR + lane * 4) = pk_lo(gq[i][0], gq[i][2]);
    }
    STAMP(3);
    BARRIER();
    return;
  }

  const int kw = wave - 1;
  const int br = kw >> 3;
  const int key0 = br ? (SK - WND + 128 * s + 64 * (kw - 8)) : (512 * s + 64 * kw);
  const size_t kvoff = ((size_t)(b * 8 + h) * SK + key0) * 256;
  const GAS unsigned char* kp = (const GAS unsigned char*)p.k[br] + kvoff + (4 * (c & 3) + (c >> 2)) * 256 + g * 16;
  const GAS unsigned char* vp = (const GAS unsigned char*)p.v[br] + kvoff + lane * 16;
  u32x4 K[16], V[16];
#pragma unroll
  for (int i = 0; i < 32; ++i) {
    if (i >= WIN) asm volatile("s_waitcnt vmcnt(%0)" :: "n"(WIN - 1) : "memory");
    if (i < 16) K[i] = ldg_stream(kp + (i >> 2) * 4096 + (i & 3) * 64);
    else V[i - 16] = ldg_stream(vp + (i - 16) * 1024);
    SCHED();
  }
  STAMP(1);
  BARRIER();
  STAMP(2);
  f32x4 S[4];
#pragma unroll
  for (int i = 0; i < 4; ++i) S[i] = f32x4{0.f, 0.f, 0.f, 0.f};
#pragma unroll
  for (int kk = 0; kk < 4; ++kk) {
    const u32x4 qf = *(LAS u32x4*)(L + L_Q + (br * 4 + (c & 3)) * QSTR + kk * 64 + g * 16);
#pragma unroll
    for (int i = 0; i < 4; ++i) S[i] = mfma32(K[i * 4 + kk], qf, S[i]);
  }
  float sc[16];
  float mx = -INFINITY;
#pragma unroll
  for (int i = 0; i < 4; ++i)
#pragma unroll
    for (int t = 0; t < 2; ++t) {
      const u32 r1 = cvt_pk(S[i][2 * t], S[i][2 * t + 1]);
      const u32 r2 = cvt_pk(bf_lo(r1) * 0.08838834764831845f, bf_hi(r1) * 0.08838834764831845f);
      sc[i * 4 + 2 * t] = bf_lo(r2); sc[i * 4 + 2 * t + 1] = bf_hi(r2);
      mx = fmaxf(mx, fmaxf(sc[i * 4 + 2 * t], sc[i * 4 + 2 * t + 1]));
    }
  mx = xrow_max(mx);
  if (lane < 4) *(LAS float*)(L + L_M + (kw * 4 + lane) * 4) = mx;
  u32x4 Wo[4];
  if (kw < 8) {
    const GAS unsigned char* wp = (const GAS unsigned char*)p.wo[wbr] + (size_t)((4 * h + whh) * 128 + 32 * (kw >> 1) + 4 * (kw & 1) + g) * 1024 + wnb * 256 + c * 16;
#pragma unroll
    for (int t = 0; t < 4; ++t) Wo[t] = ldg_stream(wp + t * 8192);
  }
  SCHED();
  BARRIER();
  float M;
  {
    const LAS float* mp = (const LAS float*)(L + L_M) + (c & 3);
    if (br == 0) { M = mp[0];
#pragma unroll
      for (int w = 1; w < 8; ++w) M = fmaxf(M, mp[4 * w]);
    } else M = fmaxf(mp[32], mp[36]);
  }
  constexpr float LOG2E = 1.4426950408889634f;
  const float mneg = -M * LOG2E;
  float lsum = 0.f;
  u32x4 P[2];
#pragma unroll
  for (int i = 0; i < 4; ++i)
#pragma unroll
    for (int t = 0; t < 2; ++t) {
      const float p0 = __builtin_amdgcn_exp2f(fmaf(sc[i * 4 + 2 * t], LOG2E, mneg)), p1 = __builtin_amdgcn_exp2f(fmaf(sc[i * 4 + 2 * t + 1], LOG2E, mneg));
      lsum += p0 + p1;
      P[i >> 1][(i & 1) * 2 + t] = cvt_pk(p0, p1);
    }
  lsum = xrow_sum(lsum);
  if (lane < 4) *(LAS float*)(L + L_L + (kw * 4 + lane) * 4) = lsum;
  STAMP(3);
  f32x4 O[8];
#pragma unroll
  for (int e = 0; e < 8; ++e) O[e] = f32x4{0.f, 0.f, 0.f, 0.f};
#pragma unroll
  for (int ip = 0; ip < 2; ++ip)
#pragma unroll
    for (int d = 0; d < 4; ++d) {
      u32x4 alo, ahi;
#pragma unroll
      for (int t = 0; t < 4; ++t) { alo[t] = pk_lo(V[ip * 8 + 2 * t][d], V[ip * 8 + 2 * t + 1][d]); ahi[t] = pk_hi(V[ip * 8 + 2 * t][d], V[ip * 8 + 2 * t + 1][d]); }
      O[2 * d] = mfma32(alo, P[ip], O[2 * d]);
      O[2 * d + 1] = mfma32(ahi, P[ip], O[2 * d + 1]);
    }
  if (c < 4) {
#pragma unroll
    for (int e = 0; e < 8; ++e) *(LAS f32x4*)(L + L_O + kw * OW + (c * 132 + 32 * g + 4 * e) * 4) = O[e];
  }
  STAMP(4);
  BARRIER();
  if (kw < 4) {
    const int t = kw * 64 + lane, rbr = t >> 7, hh = (t >> 5) & 3, quad = t & 31;
    const LAS unsigned char* op = L + L_O + (hh * 132 + 4 * quad) * 4;
    f32x4 a;
    if (rbr == 0) {
      a = *(const LAS f32x4*)op;
#pragma unroll
      for (int w = 1; w < 8; ++w) a += *(const LAS f32x4*)(op + w * OW);
    } else a = *(const LAS f32x4*)(op + 8 * OW) + *(const LAS f32x4*)(op + 9 * OW);
    const u32x4 o = {clean_bits(a[0]), clean_bits(a[1]), clean_bits(a[2]), clean_bits(a[3])};
    __builtin_amdgcn_raw_buffer_store_b128(o, rs, WS_R1 + (((rbr * 32 + 4 * h + hh) * 2 + b) * 16 + s) * R1_STRIDE + quad * 16, 0, AUX_SC1);
  } else if (kw == 4 || kw == 8) {
    if (lane < 4) {
      const LAS float* lp = (const LAS float*)(L + L_L) + lane;
      float l;
      if (br == 0) { l = lp[0];
#pragma unroll
        for (int w = 1; w < 8; ++w) l += lp[4 * w];
      } else l = lp[32] + lp[36];
      __builtin_amdgcn_raw_buffer_store_b64(u32x2{clean_bits(M), clean_bits(l)}, rs, WS_R1 + (((br * 32 + 4 * h + lane) * 2 + b) * 16 + s) * R1_STRIDE + 512, 0, AUX_SC1);
    }
  }
  if (kw >= 8) return;
  STAMP(5);
  u32x2 Wt[8];
#pragma unroll
  for (int d = 0; d < 4; ++d) {
    Wt[2 * d] = u32x2{pk_lo(Wo[0][d], Wo[1][d]), pk_lo(Wo[2][d], Wo[3][d])};
    Wt[2 * d + 1] = u32x2{pk_hi(Wo[0][d], Wo[1][d]), pk_hi(Wo[2][d], Wo[3][d])};
  }
  {
    const int rec0 = WS_R1 + (((wbr * 32 + 4 * h + whh) * 2 + (c & 1)) * 16 + 2 * (c >> 1)) * R1_STRIDE;
    u32x4 o0, o1; u32x2 ml0, ml1;
    for (u32 spin = 0;; ++spin) {
      o0 = __builtin_amdgcn_raw_buffer_load_b128(rs, rec0 + (16 * kw + 4 * g) * 4, 0, AUX_SC1);
      o1 = __builtin_amdgcn_raw_buffer_load_b128(rs, rec0 + R1_STRIDE + (16 * kw + 4 * g) * 4, 0, AUX_SC1);
      ml0 = __builtin_amdgcn_raw_buffer_load_b64(rs, rec0 + 512, 0, AUX_SC1);
      ml1 = __builtin_amdgcn_raw_buffer_load_b64(rs, rec0 + R1_STRIDE + 512, 0, AUX_SC1);
      const u32 mxu = umax3(umax3(umax3(o0[0], o0[1], o0[2]), umax3(o0[3], o1[0], o1[1]), umax3(o1[2], o1[3], ml0[0])), umax3(ml0[1], ml1[0], ml1[1]), 0u);
      if (__builtin_amdgcn_ballot_w64(mxu == SENT) == 0 || spin >= SPIN_CAP) break;
      __builtin_amdgcn_s_sleep(1);
    }
    STAMP(6);
    const float m0 = __uint_as_float(ml0[0]), m1 = __uint_as_float(ml1[0]);
    float Mg = fmaxf(m0, m1);
    Mg = fmaxf(Mg, dpp<DPP_ROR2>(Mg)); Mg = fmaxf(Mg, dpp<DPP_ROR4>(Mg)); Mg = fmaxf(Mg, dpp<DPP_ROR8>(Mg));
    const float w0 = __builtin_amdgcn_exp2f((m0 - Mg) * LOG2E), w1 = __builtin_amdgcn_exp2f((m1 - Mg) * LOG2E);
    float Ls = w0 * __uint_as_float(ml0[1]) + w1 * __uint_as_float(ml1[1]);
    Ls += dpp<DPP_XOR2>(Ls); Ls += dpp<DPP_ROR4>(Ls); Ls += dpp<DPP_ROR8>(Ls);
    float ov[4];
#pragma unroll
    for (int t = 0; t < 4; ++t) {
      float a = w0 * __uint_as_float(o0[t]) + w1 * __uint_as_float(o1[t]);
      a += dpp<DPP_XOR2>(a); a += dpp<DPP_ROR4>(a); a += dpp<DPP_ROR8>(a);
      ov[t] = a;
    }
    const float inv = 1.0f / Ls;
    const u32x2 oa = {cvt_pk(ov[0] * inv, ov[1] * inv), cvt_pk(ov[2] * inv, ov[3] * inv)};
    f32x4 D[8];
#pragma unroll
    for (int e = 0; e < 8; ++e) D[e] = mfma16(oa, Wt[e], f32x4{0.f, 0.f, 0.f, 0.f});
    if (g == 0) {
#pragma unroll
      for (int e = 0; e < 8; e += 2) *(LAS f32x4*)(L + L_O2 + kw * 1024 + (8 * c + e) * 8) = f32x4{D[e][0], D[e][1], D[e + 1][0], D[e + 1][1]};
    }
  }
  BARRIER();
  if (kw != 0) return;
  {
    const LAS unsigned char* op = L + L_O2 + lane * 16;
    f32x4 a = *(const LAS f32x4*)op;
#pragma unroll
    for (int w = 1; w < 8; ++w) a += *(const LAS f32x4*)(op + w * 1024);
    const u32x4 o = {clean_bits(a[0]), clean_bits(a[1]), clean_bits(a[2]), clean_bits(a[3])};
    __builtin_amdgcn_raw_buffer_store_b128(o, rs, WS_R2 + ((wbr * 4 + wnb) * 32 + 4 * h + whh) * 1024 + lane * 16, 0, AUX_SC1);
  }
  STAMP(7);
  {
    const int rbr = blockIdx.x >> 7, rnb = (blockIdx.x >> 5) & 3, cg = blockIdx.x & 31;
    const int off = WS_R2 + ((rbr * 4 + rnb) * 32 + (lane >> 1)) * 1024 + cg * 32 + (lane & 1) * 16;
    u32x4 r;
    for (u32 spin = 0;; ++spin) {
      r = __builtin_amdgcn_raw_buffer_load_b128(rs, off, 0, AUX_SC1);
      const u32 mxu = max(umax3(r[0], r[1], r[2]), r[3]);
      if (__builtin_amdgcn_ballot_w64(mxu == SENT) == 0 || spin >= SPIN_CAP) break;
      __builtin_amdgcn_s_sleep(1);
    }
    STAMP(8);
    float v[4];
#pragma unroll
    for (int t = 0; t < 4; ++t) {
      float a = __uint_as_float(r[t]);
      a += dpp<DPP_XOR2>(a); a += dpp<DPP_ROR4>(a); a += dpp<DPP_ROR8>(a);
      v[t] = xrow_sum(a);
    }
    if (lane < 2) {
      GAS u32* o = (GAS u32*)(p.out + rbr * 512 + rnb * 128 + 4 * cg + 2 * lane);
      o[0] = cvt_pk(v[0], v[2]);
      o[512] = cvt_pk(v[1], v[3]);
    }
    if (blockIdx.x == 0 && lane == 0) __hip_atomic_store((GAS u64*)(p.ws + WS_EPOCH), *(LAS u64*)(L + L_TAG), __ATOMIC_RELAXED, __HIP_MEMORY_SCOPE_AGENT);
    STAMP(9);
  }
}

extern "C" void kernel_launch(void* const* d_in, const int* in_sizes, int n_in, void* d_out, int out_size, void* d_ws, size_t ws_size, hipStream_t stream) {
  Params p;
  p.x[0] = (const u16*)d_in[0]; p.k[0] = (const u16*)d_in[1]; p.v[0] = (const u16*)d_in[2]; p.wq[0] = (const u16*)d_in[3]; p.wo[0] = (const u16*)d_in[4];
  p.x[1] = (const u16*)d_in[5]; p.k[1] = (const u16*)d_in[6]; p.v[1] = (const u16*)d_in[7]; p.wq[1] = (const u16*)d_in[8]; p.wo[1] = (const u16*)d_in[9];
  p.out = (u16*)d_out; p.ws = (unsigned char*)d_ws;
  fused_decode<<<dim3(256), dim3(NTHR), L_END, stream>>>(p);
}
```

```cpp
#define TCH 0
#include <hip/hip_runtime.h>
#include <stdint.h>

typedef unsigned short u16;
typedef unsigned int u32;
typedef unsigned long long u64;
typedef u32 u32x2 __attribute__((ext_vector_type(2)));
typedef u32 u32x4 __attribute__((ext_vector_type(4)));
typedef float f32x2 __attribute__((ext_vector_type(2)));
typedef float f32x4 __attribute__((ext_vector_type(4)));
typedef short s16x4 __attribute__((ext_vector_type(4)));
typedef __bf16 bf16x2 __attribute__((ext_vector_type(2)));
typedef __bf16 bf16x8 __attribute__((ext_vector_type(8)));
#define GAS __attribute__((address_space(1)))
#define LAS __attribute__((address_space(3)))

#define WIN 6
#define KV_NT 1
#define DIAG 0

constexpr int SK = 8192, WND = 2048;
constexpr int NWAVE = 11, NTHR = NWAVE * 64;
constexpr u32 SENT = 0xFFFFFFFFu;
constexpr int AUX_SC1 = 16;
constexpr int WS_EPOCH = 0;
constexpr int WS_QG = 4096;
constexpr int R1_STRIDE = 640;
constexpr int WS_R1 = WS_QG + 4 * 4096 * 8;
constexpr int WS_R2 = WS_R1 + 2048 * R1_STRIDE;
constexpr int WS_END = WS_R2 + 256 * 1024;
constexpr int WS_DIAG = 4 << 20;
constexpr u32 SPIN_CAP = 1u << 18;
constexpr int L_Q = 0;
constexpr int QSTR = 272;
constexpr int L_TAG = L_Q + 8 * QSTR;
constexpr int L_M = L_TAG + 16;
constexpr int L_L = L_M + 160;
constexpr int L_O = L_L + 160;
constexpr int OW = 4 * 132 * 4;
constexpr int L_O2 = L_O + 10 * OW;
constexpr int L_QR = L_O2;
constexpr int L_END = L_O2 + 8 * 1024;
static_assert(L_O % 16 == 0 && L_O2 % 16 == 0, "align");
constexpr int LDS_BYTES = 84 * 1024;

struct Params {
  const u16* x[2]; const u16* k[2]; const u16* v[2]; const u16* wq[2]; const u16* wo[2];
  u16* out; unsigned char* ws;
};

__device__ __forceinline__ u32x4 ldg_stream(const GAS unsigned char* p) {
  return __builtin_nontemporal_load((const GAS u32x4*)p);
}
__device__ __forceinline__ f32x4 mfma32(u32x4 a, u32x4 b, f32x4 c) {
  return __builtin_amdgcn_mfma_f32_16x16x32_bf16(__builtin_bit_cast(bf16x8, a), __builtin_bit_cast(bf16x8, b), c, 0, 0, 0);
}
__device__ __forceinline__ f32x4 mfma16(u32x2 a, u32x2 b, f32x4 c) {
  return __builtin_amdgcn_mfma_f32_16x16x16bf16_1k(__builtin_bit_cast(s16x4, a), __builtin_bit_cast(s16x4, b), c, 0, 0, 0);
}
__device__ __forceinline__ u32 pk_lo(u32 a, u32 b) { return __builtin_amdgcn_perm(b, a, 0x05040100u); }
__device__ __forceinline__ u32 pk_hi(u32 a, u32 b) { return __builtin_amdgcn_perm(b, a, 0x07060302u); }
__device__ __forceinline__ u32 cvt_pk(float lo, float hi) {
  f32x2 f = {lo, hi};
  return __builtin_bit_cast(u32, __builtin_convertvector(f, bf16x2));
}
__device__ __forceinline__ float bf_lo(u32 v) { return __uint_as_float(v << 16); }
__device__ __forceinline__ float bf_hi(u32 v) { return __uint_as_float(v & 0xffff0000u); }
template <int CTRL> __device__ __forceinline__ float dpp(float x) {
  return __builtin_bit_cast(float, __builtin_amdgcn_mov_dpp(__builtin_bit_cast(int, x), CTRL, 0xf, 0xf, true));
}
constexpr int DPP_XOR1 = 0xB1, DPP_XOR2 = 0x4E, DPP_ROR2 = 0x122, DPP_ROR4 = 0x124, DPP_ROR8 = 0x128;
__device__ __forceinline__ float xrow_max(float x) {
  auto s = __builtin_amdgcn_permlane16_swap(__float_as_uint(x), __float_as_uint(x), false, false);
  x = fmaxf(__uint_as_float(s[0]), __uint_as_float(s[1]));
  auto t = __builtin_amdgcn_permlane32_swap(__float_as_uint(x), __float_as_uint(x), false, false);
  return fmaxf(__uint_as_float(t[0]), __uint_as_float(t[1]));
}
__device__ __forceinline__ float xrow_sum(float x) {
  auto s = __builtin_amdgcn_permlane16_swap(__float_as_uint(x), __float_as_uint(x), false, false);
  x = __uint_as_float(s[0]) + __uint_as_float(s[1]);
  auto t = __builtin_amdgcn_permlane32_swap(__float_as_uint(x), __float_as_uint(x), false, false);
  return __uint_as_float(t[0]) + __uint_as_float(t[1]);
}
__device__ __forceinline__ u32 clean_bits(float f) { u32 u = __float_as_uint(f); return u < 0xFFFFFFFEu ? u : 0xFFFFFFFEu; }
__device__ __forceinline__ u32 umax3(u32 a, u32 b, u32 c) { return max(max(a, b), c); }
#define SCHED() __builtin_amdgcn_sched_barrier(0)
#define BARRIER() do { asm volatile("s_waitcnt lgkmcnt(0)" ::: "memory"); __builtin_amdgcn_s_barrier(); asm volatile("" ::: "memory"); } while (0)
#define STAMP(i) do { if ((i) == 0) tch0 = __builtin_amdgcn_s_memrealtime(); if ((i) == TCH) tch1 = __builtin_amdgcn_s_memrealtime(); } while (0)

__global__ void __launch_bounds__(NTHR, 3) fused_decode(Params p) {
  extern __shared__ __attribute__((aligned(16))) unsigned char smem[];
  LAS unsigned char* L = (LAS unsigned char*)smem;
  const int tid = threadIdx.x, lane = tid & 63;
  const int wave = __builtin_amdgcn_readfirstlane(tid >> 6);
  const int g = lane >> 4, c = lane & 15;
  const int h = blockIdx.x & 7, j = blockIdx.x >> 3;
  const int b = j >> 4, s = j & 15;
  const int wbr = j >> 4, whh = (j >> 2) & 3, wnb = j & 3;
  const __amdgpu_buffer_rsrc_t rs = __builtin_amdgcn_make_buffer_rsrc((void*)p.ws, 0, WS_END, 0x00020000);
  u64 tch0 = 0, tch1 = 0;
  STAMP(0);

  if (wave == 0) {
    const u64 E = __hip_atomic_load((const GAS u64*)(p.ws + WS_EPOCH), __ATOMIC_RELAXED, __HIP_MEMORY_SCOPE_AGENT);
    const int qbr = j >> 4, qc0 = 512 * h + 32 * (j & 15);
    const GAS unsigned char* wq = (const GAS unsigned char*)p.wq[qbr] + (size_t)qc0 * 2 + (size_t)(8 * (lane >> 2)) * 8192 + (lane & 3) * 16;
    const GAS unsigned char* xp = (const GAS unsigned char*)p.x[qbr] + (c & 1) * 1024 + (4 * g + (c >> 2)) * 16;
    u32x4 xa[4], w[32];
#pragma unroll
    for (int i = 0; i < 4; ++i) xa[i] = *(const GAS u32x4*)(xp + i * 256);
#pragma unroll
    for (int i = 0; i < 24; ++i) w[i] = ldg_stream(wq + (size_t)((i >> 3) * 128 + (i & 7)) * 8192);
    SCHED();
    {
      const u32x4 sv = {SENT, SENT, SENT, SENT};
#pragma unroll
      for (int i = 0; i < 4; ++i) {
        const int rec = 2 * i + (lane >> 5);
        const int off = WS_R1 + ((((rec >> 2) * 32 + 4 * h + (rec & 3)) * 2 + b) * 16 + s) * R1_STRIDE + (lane & 31) * 16;
        __builtin_amdgcn_raw_buffer_store_b128(sv, rs, off, 0, AUX_SC1);
      }
      if (lane < 8) {
        const int off = WS_R1 + ((((lane >> 2) * 32 + 4 * h + (lane & 3)) * 2 + b) * 16 + s) * R1_STRIDE + 512;
        __builtin_amdgcn_raw_buffer_store_b128(sv, rs, off, 0, AUX_SC1);
      }
      __builtin_amdgcn_raw_buffer_store_b128(sv, rs, WS_R2 + ((wbr * 4 + wnb) * 32 + 4 * h + whh) * 1024 + lane * 16, 0, AUX_SC1);
    }
    SCHED();
    STAMP(1);
    f32x4 acc[8];
#pragma unroll
    for (int e = 0; e < 8; ++e) acc[e] = f32x4{0.f, 0.f, 0.f, 0.f};
#pragma unroll
    for (int gi = 0; gi < 4; ++gi) {
#pragma unroll
      for (int d = 0; d < 4; ++d) {
        u32x4 blo, bhi;
#pragma unroll
        for (int t = 0; t < 4; ++t) { blo[t] = pk_lo(w[gi * 8 + 2 * t][d], w[gi * 8 + 2 * t + 1][d]); bhi[t] = pk_hi(w[gi * 8 + 2 * t][d], w[gi * 8 + 2 * t + 1][d]); }
        acc[2 * d] = mfma32(xa[gi], blo, acc[2 * d]);
        acc[2 * d + 1] = mfma32(xa[gi], bhi, acc[2 * d + 1]);
      }
      if (gi == 0) {
        SCHED();
#pragma unroll
        for (int i = 24; i < 32; ++i) w[i] = ldg_stream(wq + (size_t)((i >> 3) * 128 + (i & 7)) * 8192);
        SCHED();
      }
    }
    LAS float* qr = (LAS float*)(L + L_QR);
    if (g == (c >> 2)) {
#pragma unroll
      for (int e = 0; e < 8; ++e) *(LAS f32x2*)(qr + (g * 32 + 8 * (c & 3) + e) * 2) = f32x2{acc[e][0], acc[e][1]};
    }
    asm volatile("s_waitcnt vmcnt(0) lgkmcnt(0)" ::: "memory");
    u64 z = E + 0x9E3779B97F4A7C15ull;
    z = (z ^ (z >> 30)) * 0xBF58476D1CE4E5B9ull; z = (z ^ (z >> 27)) * 0x94D049BB133111EBull; z ^= z >> 31;
    const u32 tlo = (u32)z & 0xFFFF0000u, thi = (u32)(z >> 32);
    {
      const int col = lane & 31, bb = lane >> 5;
      const float qv = (qr[(0 * 32 + col) * 2 + bb] + qr[(1 * 32 + col) * 2 + bb]) + (qr[(2 * 32 + col) * 2 + bb] + qr[(3 * 32 + col) * 2 + bb]);
      const u32 qb = cvt_pk(qv, qv) & 0xFFFFu;
      __builtin_amdgcn_raw_buffer_store_b64(u32x2{qb | tlo, thi}, rs, WS_QG + ((qbr * 2 + bb) * 4096 + qc0 + col) * 8, 0, AUX_SC1);
    }
    STAMP(2);
    if (lane == 0) *(LAS u64*)(L + L_TAG) = z;
    {
      const int rbr = blockIdx.x >> 7, rnb = (blockIdx.x >> 5) & 3;
      const int pj = rbr * 16 + (lane & 3) * 4 + rnb, ph = (lane >> 2) & 7;
      const int roff = WS_QG + (((pj >> 4) * 2) * 4096 + 512 * ph + 32 * (pj & 15)) * 8;
      u32x4 gq[8];
      for (u32 spin = 0;; ++spin) {
        const u32x2 rdy = __builtin_amdgcn_raw_buffer_load_b64(rs, roff, 0, AUX_SC1);
#pragma unroll
        for (int i = 0; i < 8; ++i) gq[i] = __builtin_amdgcn_raw_buffer_load_b128(rs, WS_QG + (((i >> 2) * 2 + b) * 4096 + 512 * h + (i & 3) * 128 + lane * 2) * 8, 0, AUX_SC1);
        u32 bad = ((rdy[0] ^ tlo) & 0xFFFF0000u) | (rdy[1] ^ thi);
#pragma unroll
        for (int i = 0; i < 8; ++i) bad |= (((gq[i][0] ^ tlo) | (gq[i][2] ^ tlo)) & 0xFFFF0000u) | (gq[i][1] ^ thi) | (gq[i][3] ^ thi);
        if (__builtin_amdgcn_ballot_w64(bad != 0) == 0 || spin >= SPIN_CAP) break;
        __builtin_amdgcn_s_sleep(2);
      }
#pragma unroll
      for (int i = 0; i < 8; ++i) *(LAS u32*)(L + L_Q + i * QSTR + lane * 4) = pk_lo(gq[i][0], gq[i][2]);
    }
    STAMP(3);
    BARRIER();
    return;
  }

  const int kw = wave - 1;
  const int br = kw >> 3;
  const int key0 = br ? (SK - WND + 128 * s + 64 * (kw - 8)) : (512 * s + 64 * kw);
  const size_t kvoff = ((size_t)(b * 8 + h) * SK + key0) * 256;
  const GAS unsigned char* kp = (const GAS unsigned char*)p.k[br] + kvoff + (4 * (c & 3) + (c >> 2)) * 256 + g * 16;
  const GAS unsigned char* vp = (const GAS unsigned char*)p.v[br] + kvoff + lane * 16;
  u32x4 K[16], V[16];
#pragma unroll
  for (int i = 0; i < 32; ++i) {
    if (i >= WIN) asm volatile("s_waitcnt vmcnt(%0)" :: "n"(WIN - 1) : "memory");
    if (i == WIN) STAMP(10);
    if (i < 16) K[i] = ldg_stream(kp + (i >> 2) * 4096 + (i & 3) * 64);
    else V[i - 16] = ldg_stream(vp + (i - 16) * 1024);
    SCHED();
  }
  STAMP(1);
  BARRIER();
  STAMP(2);
  f32x4 S[4];
#pragma unroll
  for (int i = 0; i < 4; ++i) S[i] = f32x4{0.f, 0.f, 0.f, 0.f};
#pragma unroll
  for (int kk = 0; kk < 4; ++kk) {
    const u32x4 qf = *(LAS u32x4*)(L + L_Q + (br * 4 + (c & 3)) * QSTR + kk * 64 + g * 16);
#pragma unroll
    for (int i = 0; i < 4; ++i) S[i] = mfma32(K[i * 4 + kk], qf, S[i]);
  }
  float sc[16];
  float mx = -INFINITY;
#pragma unroll
  for (int i = 0; i < 4; ++i)
#pragma unroll
    for (int t = 0; t < 2; ++t) {
      const u32 r1 = cvt_pk(S[i][2 * t], S[i][2 * t + 1]);
      const u32 r2 = cvt_pk(bf_lo(r1) * 0.08838834764831845f, bf_hi(r1) * 0.08838834764831845f);
      sc[i * 4 + 2 * t] = bf_lo(r2); sc[i * 4 + 2 * t + 1] = bf_hi(r2);
      mx = fmaxf(mx, fmaxf(sc[i * 4 + 2 * t], sc[i * 4 + 2 * t + 1]));
    }
  mx = xrow_max(mx);
  if (lane < 4) *(LAS float*)(L + L_M + (kw * 4 + lane) * 4) = mx;
  u32x4 Wo[4];
  if (kw < 8) {
    const GAS unsigned char* wp = (const GAS unsigned char*)p.wo[wbr] + (size_t)((4 * h + whh) * 128 + 32 * (kw >> 1) + 4 * (kw & 1) + g) * 1024 + wnb * 256 + c * 16;
#pragma unroll
    for (int t = 0; t < 4; ++t) Wo[t] = ldg_stream(wp + t * 8192);
  }
  SCHED();
  BARRIER();
  float M;
  {
    const LAS float* mp = (const LAS float*)(L + L_M) + (c & 3);
    if (br == 0) { M = mp[0];
#pragma unroll
      for (int w = 1; w < 8; ++w) M = fmaxf(M, mp[4 * w]);
    } else M = fmaxf(mp[32], mp[36]);
  }
  constexpr float LOG2E = 1.4426950408889634f;
  const float mneg = -M * LOG2E;
  float lsum = 0.f;
  u32x4 P[2];
#pragma unroll
  for (int i = 0; i < 4; ++i)
#pragma unroll
    for (int t = 0; t < 2; ++t) {
      const float p0 = __builtin_amdgcn_exp2f(fmaf(sc[i * 4 + 2 * t], LOG2E, mneg)), p1 = __builtin_amdgcn_exp2f(fmaf(sc[i * 4 + 2 * t + 1], LOG2E, mneg));
      lsum += p0 + p1;
      P[i >> 1][(i & 1) * 2 + t] = cvt_pk(p0, p1);
    }
  lsum = xrow_sum(lsum);
  if (lane < 4) *(LAS float*)(L + L_L + (kw * 4 + lane) * 4) = lsum;
  STAMP(3);
  f32x4 O[8];
#pragma unroll
  for (int e = 0; e < 8; ++e) O[e] = f32x4{0.f, 0.f, 0.f, 0.f};
#pragma unroll
  for (int ip = 0; ip < 2; ++ip)
#pragma unroll
    for (int d = 0; d < 4; ++d) {
      u32x4 alo, ahi;
#pragma unroll
      for (int t = 0; t < 4; ++t) { alo[t] = pk_lo(V[ip * 8 + 2 * t][d], V[ip * 8 + 2 * t + 1][d]); ahi[t] = pk_hi(V[ip * 8 + 2 * t][d], V[ip * 8 + 2 * t + 1][d]); }
      O[2 * d] = mfma32(alo, P[ip], O[2 * d]);
      O[2 * d + 1] = mfma32(ahi, P[ip], O[2 * d + 1]);
    }
  if (c < 4) {
#pragma unroll
    for (int e = 0; e < 8; ++e) *(LAS f32x4*)(L + L_O + kw * OW + (c * 132 + 32 * g + 4 * e) * 4) = O[e];
  }
  STAMP(4);
  BARRIER();
  if (kw < 4) {
    const int t = kw * 64 + lane, rbr = t >> 7, hh = (t >> 5) & 3, quad = t & 31;
    const LAS unsigned char* op = L + L_O + (hh * 132 + 4 * quad) * 4;
    f32x4 a;
    if (rbr == 0) {
      a = *(const LAS f32x4*)op;
#pragma unroll
      for (int w = 1; w < 8; ++w) a += *(const LAS f32x4*)(op + w * OW);
    } else a = *(const LAS f32x4*)(op + 8 * OW) + *(const LAS f32x4*)(op + 9 * OW);
    const u32x4 o = {clean_bits(a[0]), clean_bits(a[1]), clean_bits(a[2]), clean_bits(a[3])};
    __builtin_amdgcn_raw_buffer_store_b128(o, rs, WS_R1 + (((rbr * 32 + 4 * h + hh) * 2 + b) * 16 + s) * R1_STRIDE + quad * 16, 0, AUX_SC1);
  } else if (kw == 4 || kw == 8) {
    if (lane < 4) {
      const LAS float* lp = (const LAS float*)(L + L_L) + lane;
      float l;
      if (br == 0) { l = lp[0];
#pragma unroll
        for (int w = 1; w < 8; ++w) l += lp[4 * w];
      } else l = lp[32] + lp[36];
      __builtin_amdgcn_raw_buffer_store_b64(u32x2{clean_bits(M), clean_bits(l)}, rs, WS_R1 + (((br * 32 + 4 * h + lane) * 2 + b) * 16 + s) * R1_STRIDE + 512, 0, AUX_SC1);
    }
  }
  if (kw >= 8) return;
  STAMP(5);
  u32x2 Wt[8];
#pragma unroll
  for (int d = 0; d < 4; ++d) {
    Wt[2 * d] = u32x2{pk_lo(Wo[0][d], Wo[1][d]), pk_lo(Wo[2][d], Wo[3][d])};
    Wt[2 * d + 1] = u32x2{pk_hi(Wo[0][d], Wo[1][d]), pk_hi(Wo[2][d], Wo[3][d])};
  }
  {
    const int rec0 = WS_R1 + (((wbr * 32 + 4 * h + whh) * 2 + (c & 1)) * 16 + 2 * (c >> 1)) * R1_STRIDE;
    u32x4 o0, o1; u32x2 ml0, ml1;
    for (u32 spin = 0;; ++spin) {
      o0 = __builtin_amdgcn_raw_buffer_load_b128(rs, rec0 + (16 * kw + 4 * g) * 4, 0, AUX_SC1);
      o1 = __builtin_amdgcn_raw_buffer_load_b128(rs, rec0 + R1_STRIDE + (16 * kw + 4 * g) * 4, 0, AUX_SC1);
      ml0 = __builtin_amdgcn_raw_buffer_load_b64(rs, rec0 + 512, 0, AUX_SC1);
      ml1 = __builtin_amdgcn_raw_buffer_load_b64(rs, rec0 + R1_STRIDE + 512, 0, AUX_SC1);
      const u32 mxu = umax3(umax3(umax3(o0[0], o0[1], o0[2]), umax3(o0[3], o1[0], o1[1]), umax3(o1[2], o1[3], ml0[0])), umax3(ml0[1], ml1[0], ml1[1]), 0u);
      if (__builtin_amdgcn_ballot_w64(mxu == SENT) == 0 || spin >= SPIN_CAP) break;
      __builtin_amdgcn_s_sleep(1);
    }
    STAMP(6);
    const float m0 = __uint_as_float(ml0[0]), m1 = __uint_as_float(ml1[0]);
    float Mg = fmaxf(m0, m1);
    Mg = fmaxf(Mg, dpp<DPP_ROR2>(Mg)); Mg = fmaxf(Mg, dpp<DPP_ROR4>(Mg)); Mg = fmaxf(Mg, dpp<DPP_ROR8>(Mg));
    const float w0 = __builtin_amdgcn_exp2f((m0 - Mg) * LOG2E), w1 = __builtin_amdgcn_exp2f((m1 - Mg) * LOG2E);
    float Ls = w0 * __uint_as_float(ml0[1]) + w1 * __uint_as_float(ml1[1]);
    Ls += dpp<DPP_XOR2>(Ls); Ls += dpp<DPP_ROR4>(Ls); Ls += dpp<DPP_ROR8>(Ls);
    float ov[4];
#pragma unroll
    for (int t = 0; t < 4; ++t) {
      float a = w0 * __uint_as_float(o0[t]) + w1 * __uint_as_float(o1[t]);
      a += dpp<DPP_XOR2>(a); a += dpp<DPP_ROR4>(a); a += dpp<DPP_ROR8>(a);
      ov[t] = a;
    }
    const float inv = 1.0f / Ls;
    const u32x2 oa = {cvt_pk(ov[0] * inv, ov[1] * inv), cvt_pk(ov[2] * inv, ov[3] * inv)};
    f32x4 D[8];
#pragma unroll
    for (int e = 0; e < 8; ++e) D[e] = mfma16(oa, Wt[e], f32x4{0.f, 0.f, 0.f, 0.f});
    if (g == 0) {
#pragma unroll
      for (int e = 0; e < 8; e += 2) *(LAS f32x4*)(L + L_O2 + kw * 1024 + (8 * c + e) * 8) = f32x4{D[e][0], D[e][1], D[e + 1][0], D[e + 1][1]};
    }
  }
  BARRIER();
  if (kw != 0) return;
  {
    const LAS unsigned char* op = L + L_O2 + lane * 16;
    f32x4 a = *(const LAS f32x4*)op;
#pragma unroll
    for (int w = 1; w < 8; ++w) a += *(const LAS f32x4*)(op + w * 1024);
    const u32x4 o = {clean_bits(a[0]), clean_bits(a[1]), clean_bits(a[2]), clean_bits(a[3])};
    __builtin_amdgcn_raw_buffer_store_b128(o, rs, WS_R2 + ((wbr * 4 + wnb) * 32 + 4 * h + whh) * 1024 + lane * 16, 0, AUX_SC1);
  }
  STAMP(7);
  {
    const int rbr = blockIdx.x >> 7, rnb = (blockIdx.x >> 5) & 3, cg = blockIdx.x & 31;
    const int off = WS_R2 + ((rbr * 4 + rnb) * 32 + (lane >> 1)) * 1024 + cg * 32 + (lane & 1) * 16;
    u32x4 r;
    for (u32 spin = 0;; ++spin) {
      r = __builtin_amdgcn_raw_buffer_load_b128(rs, off, 0, AUX_SC1);
      const u32 mxu = max(umax3(r[0], r[1], r[2]), r[3]);
      if (__builtin_amdgcn_ballot_w64(mxu == SENT) == 0 || spin >= SPIN_CAP) break;
      __builtin_amdgcn_s_sleep(1);
    }
    STAMP(8);
    float v[4];
#pragma unroll
    for (int t = 0; t < 4; ++t) {
      float a = __uint_as_float(r[t]);
      a += dpp<DPP_XOR2>(a); a += dpp<DPP_ROR4>(a); a += dpp<DPP_ROR8>(a);
      v[t] = xrow_sum(a);
    }
    if (lane < 2) {
      GAS u32* o = (GAS u32*)(p.out + rbr * 512 + rnb * 128 + 4 * cg + 2 * lane);
      o[0] = cvt_pk(v[0], v[2]);
      o[512] = cvt_pk(v[1], v[3]);
    }
    if (blockIdx.x == 0 && lane == 0) __hip_atomic_store((GAS u64*)(p.ws + WS_EPOCH), *(LAS u64*)(L + L_TAG), __ATOMIC_RELAXED, __HIP_MEMORY_SCOPE_AGENT);
    STAMP(9);
    if (blockIdx.x == 0) { const u64 tn = __builtin_amdgcn_s_memrealtime(); const u64 until = tn + (tch1 - tch0); while (__builtin_amdgcn_s_memrealtime() < until) __builtin_amdgcn_s_sleep(1); }
  }
}

extern "C" void kernel_launch(void* const* d_in, const int* in_sizes, int n_in, void* d_out, int out_size, void* d_ws, size_t ws_size, hipStream_t stream) {
  Params p;
  p.x[0] = (const u16*)d_in[0]; p.k[0] = (const u16*)d_in[1]; p.v[0] = (const u16*)d_in[2]; p.wq[0] = (const u16*)d_in[3]; p.wo[0] = (const u16*)d_in[4];
  p.x[1] = (const u16*)d_in[5]; p.k[1] = (const u16*)d_in[6]; p.v[1] = (const u16*)d_in[7]; p.wq[1] = (const u16*)d_in[8]; p.wo[1] = (const u16*)d_in[9];
  p.out = (u16*)d_out; p.ws = (unsigned char*)d_ws;
  fused_decode<<<dim3(256), dim3(NTHR), L_END, stream>>>(p);
}
```

```cpp
#include <hip/hip_runtime.h>
#include <stdint.h>

typedef unsigned short u16;
typedef unsigned int u32;
typedef unsigned long long u64;
typedef u32 u32x2 __attribute__((ext_vector_type(2)));
typedef u32 u32x4 __attribute__((ext_vector_type(4)));
typedef float f32x2 __attribute__((ext_vector_type(2)));
typedef float f32x4 __attribute__((ext_vector_type(4)));
typedef __bf16 bf16x2 __attribute__((ext_vector_type(2)));
typedef __bf16 bf16x8 __attribute__((ext_vector_type(8)));
typedef __amdgpu_buffer_rsrc_t rsrc_t;
#define GAS __attribute__((address_space(1)))
#define LAS __attribute__((address_space(3)))
#define CAS __attribute__((address_space(4)))

constexpr int CODE_PF = 12160;
constexpr int WIN = 4;
constexpr int KPRE = 4;
constexpr int TAU = 403;
constexpr int PT0 = 100;
constexpr int TEND = PT0 + (32 - KPRE) * TAU / 10;
constexpr int TAUW = 370;
constexpr int WABS = 14;
constexpr int QT = 410;
constexpr int ODT = 55;
constexpr int OLATE = 220;
constexpr int P2DELAY = 19;
constexpr int V1N = 8, VBURST = 2;
constexpr int AUX_NT = 2, AUX_SC1 = 16;

constexpr int SK = 8192, WND = 2048;
constexpr int NTHR = 1024;
constexpr int WS_EPOCH = 0;
constexpr int WS_QG = 4096;
constexpr int WS_ML = WS_QG + 8 * 4096 * 8;
constexpr int WS_R1 = WS_ML + 256 * 128;
constexpr int R1S = 16 * 512 + 128;
constexpr int R2S = 4096 + 128;
constexpr int WS_R2 = WS_R1 + 128 * R1S;
constexpr int WS_END = WS_R2 + 256 * R2S;
constexpr u32 SPIN_CAP = 1u << 18;
constexpr int QSTR = 272;
constexpr int L_Q = 0;
constexpr int L_CTL = L_Q + 8 * QSTR;
constexpr int L_ZERO = L_CTL + 32;
constexpr int L_TAG = L_ZERO + 16;
constexpr int L_ML = L_TAG + 16;
constexpr int L_A = L_ML + 320;
constexpr int L_O = L_A + 128;
constexpr int OW = 4 * 132 * 4;
constexpr int L_QR = L_O + 10 * OW;
constexpr int L_FW = L_QR + 2048;
constexpr int L_F = L_FW + 256;
constexpr int L_END = L_F + 1024;
static_assert(L_O % 16 == 0 && L_A % 16 == 0 && L_QR % 16 == 0 && L_CTL % 16 == 0, "align");
constexpr int C_Q = L_CTL, C_O = L_CTL + 4, C_H = L_CTL + 8, C_ML = L_CTL + 12, C_ISS = L_CTL + 16;

struct Params {
  const u16* x[2]; const u16* k[2]; const u16* v[2]; const u16* wq[2]; const u16* wo[2];
  u16* out; unsigned char* ws;
};

__device__ __forceinline__ rsrc_t mk_rsrc(const void* p, int bytes) { return __builtin_amdgcn_make_buffer_rsrc((void*)p, 0, bytes, 0x00020000); }
__device__ __forceinline__ f32x4 mfma32(u32x4 a, u32x4 b, f32x4 c) {
  return __builtin_amdgcn_mfma_f32_16x16x32_bf16(__builtin_bit_cast(bf16x8, a), __builtin_bit_cast(bf16x8, b), c, 0, 0, 0);
}
__device__ __forceinline__ u32 pk_lo(u32 a, u32 b) { return __builtin_amdgcn_perm(b, a, 0x05040100u); }
__device__ __forceinline__ u32 pk_hi(u32 a, u32 b) { return __builtin_amdgcn_perm(b, a, 0x07060302u); }
__device__ __forceinline__ u32 cvt_pk(float lo, float hi) {
  f32x2 f = {lo, hi};
  return __builtin_bit_cast(u32, __builtin_convertvector(f, bf16x2));
}
__device__ __forceinline__ float bf_lo(u32 v) { return __uint_as_float(v << 16); }
__device__ __forceinline__ float bf_hi(u32 v) { return __uint_as_float(v & 0xffff0000u); }
template <int CTRL> __device__ __forceinline__ float dpp(float x) {
  return __builtin_bit_cast(float, __builtin_amdgcn_mov_dpp(__builtin_bit_cast(int, x), CTRL, 0xf, 0xf, true));
}
constexpr int DPP_XOR1 = 0xB1, DPP_XOR2 = 0x4E, DPP_HMIR = 0x141, DPP_ROR4 = 0x124, DPP_ROR8 = 0x128;
template <int D> __device__ __forceinline__ float from_bank(float x) {
  if constexpr ((D & 3) == 0) return x; else return dpp<0x120 + 16 - 4 * (D & 3)>(x);
}
template <int BANK> __device__ __forceinline__ float merge_bank(float old, float x) {
  return __builtin_bit_cast(float, __builtin_amdgcn_update_dpp(__builtin_bit_cast(int, old), __builtin_bit_cast(int, x), 0xE4, 0xf, 1 << BANK, false));
}
template <int FROM, int TO> __device__ __forceinline__ u32 to_bank(u32 x) {
  constexpr int K = (4 * (TO - FROM)) & 15;
  return (u32)__builtin_amdgcn_update_dpp(0, (int)x, K ? 0x120 + K : 0xE4, 0xf, 1 << TO, false);
}
template <int ROW> __device__ __forceinline__ float merge_row(float old, float x) {
  return __builtin_bit_cast(float, __builtin_amdgcn_update_dpp(__builtin_bit_cast(int, old), __builtin_bit_cast(int, x), 0xE4, 1 << ROW, 0xf, false));
}
__device__ __forceinline__ float xrow_max(float x) {
  auto s = __builtin_amdgcn_permlane16_swap(__float_as_uint(x), __float_as_uint(x), false, false);
  x = fmaxf(__uint_as_float(s[0]), __uint_as_float(s[1]));
  auto t = __builtin_amdgcn_permlane32_swap(__float_as_uint(x), __float_as_uint(x), false, false);
  return fmaxf(__uint_as_float(t[0]), __uint_as_float(t[1]));
}
__device__ __forceinline__ float xrow_sum(float x) {
  auto s = __builtin_amdgcn_permlane16_swap(__float_as_uint(x), __float_as_uint(x), false, false);
  x = __uint_as_float(s[0]) + __uint_as_float(s[1]);
  auto t = __builtin_amdgcn_permlane32_swap(__float_as_uint(x), __float_as_uint(x), false, false);
  return __uint_as_float(t[0]) + __uint_as_float(t[1]);
}
__device__ __forceinline__ float reduce8x4(f32x4 a) {
  const auto s0 = __builtin_amdgcn_permlane16_swap(__float_as_uint(a[0]), __float_as_uint(a[1]), false, false);
  const auto s1 = __builtin_amdgcn_permlane16_swap(__float_as_uint(a[2]), __float_as_uint(a[3]), false, false);
  const float u = __uint_as_float(s0[0]) + __uint_as_float(s0[1]), v = __uint_as_float(s1[0]) + __uint_as_float(s1[1]);
  const auto s2 = __builtin_amdgcn_permlane32_swap(__float_as_uint(u), __float_as_uint(v), false, false);
  const float w = __uint_as_float(s2[0]) + __uint_as_float(s2[1]);
  return w + dpp<DPP_ROR8>(w);
}
template <u32 VM> __device__ __forceinline__ u32x4 gran2(float a, float b, u32 tl, u32 th) {
  return u32x4{(__float_as_uint(a) & VM) | (tl & ~VM), th, (__float_as_uint(b) & VM) | (tl & ~VM), th};
}
template <u32 VM> __device__ __forceinline__ f32x2 gval(u32x4 g) { return f32x2{__uint_as_float(g[0] & VM), __uint_as_float(g[2] & VM)}; }
template <u32 VM> __device__ __forceinline__ u32 gbad(u32x4 g, u32 tl, u32 th) { return (((g[0] ^ tl) | (g[2] ^ tl)) & ~VM) | (g[1] ^ th) | (g[3] ^ th); }
constexpr u32 M24 = 0xFFFFFF00u, M28 = 0xFFFFFFF0u;
__device__ __forceinline__ u32x4 pack4(f32x4 a, u32 ta, u32 tb) {
  u32x4 r;
#pragma unroll
  for (int i = 0; i < 4; ++i) r[i] = ((__float_as_uint(a[i]) + 0x800u) & 0xFFFFF000u) | ((i & 1) ? tb : ta);
  return r;
}
__device__ __forceinline__ f32x4 val4(u32x4 g) { return __builtin_bit_cast(f32x4, g & 0xFFFFF000u); }
__device__ __forceinline__ bool bad8(u32x4 a, u32x4 b, u32 ta, u32 tb) {
  return (((((a[0] ^ ta) | (a[1] ^ tb)) | ((a[2] ^ ta) | (a[3] ^ tb))) | (((b[0] ^ ta) | (b[1] ^ tb)) | ((b[2] ^ ta) | (b[3] ^ tb)))) & 0xFFFu) != 0;
}
__device__ __forceinline__ void transpose8(const u32x4 (&w)[8], u32x4 (&t)[8]) {
#pragma unroll
  for (int d = 0; d < 4; ++d)
#pragma unroll
    for (int i = 0; i < 4; ++i) { t[2 * d][i] = pk_lo(w[2 * i][d], w[2 * i + 1][d]); t[2 * d + 1][i] = pk_hi(w[2 * i][d], w[2 * i + 1][d]); }
}
__device__ __forceinline__ u32 now32() { return (u32)__builtin_amdgcn_s_memrealtime(); }
#define SCHED() __builtin_amdgcn_sched_barrier(0)
#define MEMFENCE() asm volatile("" ::: "memory")
#define BARRIER() do { asm volatile("s_waitcnt lgkmcnt(0)" ::: "memory"); __builtin_amdgcn_s_barrier(); MEMFENCE(); } while (0)
#define LDS_LOAD(off) __hip_atomic_load((LAS u32*)(L + (off)), __ATOMIC_RELAXED, __HIP_MEMORY_SCOPE_WORKGROUP)
#define LDS_GET(off) ((u32)__builtin_amdgcn_readfirstlane(LDS_LOAD(off)))
#define LDS_WAIT(off, n) do { _Pragma("nounroll") for (u32 spin_ = 0; spin_ < (SPIN_CAP << 6); ++spin_) { if (LDS_GET(off) >= (n)) break; __builtin_amdgcn_s_sleep(1); } MEMFENCE(); } while (0)
#define LDS_ADD(off, n) do { MEMFENCE(); if (lane == 0) asm volatile("ds_add_u32 %0, %1 offset:%c2" :: "v"(lds0), "v"(n), "i"(off) : "memory"); } while (0)
#define LDS_INC(off) LDS_ADD(off, 1u)
constexpr u32 O_ALL = 9 + 3 * 256;
#define WAIT_UNTIL(t, slp) do { while ((int)(now32() - (t)) < 0) __builtin_amdgcn_s_sleep(slp); } while (0)
constexpr float LOG2E = 1.4426950408889634f;
constexpr float SCALE = 0.08838834764831845f;

__global__ void __launch_bounds__(NTHR, 4) fused_decode(Params p) {
  extern __shared__ __attribute__((aligned(16))) unsigned char smem[];
  LAS unsigned char* L = (LAS unsigned char*)smem;
  u32 pf;
  {
    u64 pc;
    asm volatile("s_getpc_b64 %0" : "=s"(pc));
    pf = *(const GAS u32*)((pc & ~255ull) + min((int)threadIdx.x * 16, CODE_PF - 4));
  }
  if (threadIdx.x < 3) *(LAS u32x4*)(L + L_CTL + threadIdx.x * 16) = u32x4{0, 0, 0, 0};
  const u16 *xA = p.x[0], *xB = p.x[1], *kA = p.k[0], *kB = p.k[1], *vA = p.v[0], *vB = p.v[1], *wqA = p.wq[0], *wqB = p.wq[1], *woA = p.wo[0], *woB = p.wo[1];
  u16* outp = p.out; unsigned char* ws = p.ws;
  const int tid = threadIdx.x, lane = tid & 63;
  const int wave = __builtin_amdgcn_readfirstlane(tid >> 6);
  const int g = lane >> 4, c = lane & 15;
  const u32 lds0 = (u32)(uintptr_t)L;
  const int blk = blockIdx.x;
  const int h = blk & 7;
  const int j = blk >> 3;
  const int b = j >> 4, s = j & 15;
  const int wbr = h >> 2, whead = 8 * ((h + 1 + 2 * wbr) & 3) + (j >> 2), wqd = j & 3;
  const int qbr = j >> 4, qh = ((h + 2) & 6) | (j & 1), qcb = ((j >> 1) & 6) | (~h & 1), qkh = (j >> 1) & 1;
  const int kw = wave - 1;
  const int br = (kw >> 3) & 1;
  int toff[4];
  {
    const int sh = 2 * br, idx0 = ((s * 8) >> sh) + (kw & 7), cst = 11 * (h + 8 * b);
#pragma unroll
    for (int i = 0; i < 4; ++i) toff[i] = ((((idx0 + ((i * 128) >> sh)) * 37 + cst) & (511 >> sh)) + br * ((SK - WND) / 16) + (b * 8 + h) * (SK / 16)) * 4096 + ((kw + s + h) & 3) * 1024;
  }
  int kvo = (c >> 2) * 256 + g * 64 + (c & 3) * 16, vvo = lane * 16;
  int wqv = (qkh * 256 + kw * 64 + (lane & 56)) * 8192 + qh * 1024 + qcb * 128 + (lane & 7) * 16;
  int xv = (c & 1) * 1024 + (qkh * 256 + kw * 64 + 16 * g + 4 * (c & 2)) * 2;
  asm volatile("" : "+s"(toff[0]), "+s"(toff[1]), "+s"(toff[2]), "+s"(toff[3]));
  asm volatile("" : "+v"(kvo), "+v"(vvo), "+v"(wqv), "+v"(xv));
  SCHED();
  asm volatile("" : "+s"(xA), "+s"(xB), "+s"(kA), "+s"(kB), "+s"(vA), "+s"(vB), "+s"(wqA), "+s"(wqB), "+s"(woA), "+s"(woB), "+s"(outp), "+s"(ws));
  const u64 t64 = __builtin_amdgcn_s_memrealtime();
  const u32 t0 = (u32)t64;
  const rsrc_t rs = mk_rsrc(ws, WS_END);

  if (wave == 0 || wave == 11) {
    const int X = wave ? 1 : 0;
    BARRIER();
    __builtin_amdgcn_s_setprio(3);
    const u64 E = *(const CAS u64*)(ws + WS_EPOCH);
    u64 z = E + 0x9E3779B97F4A7C15ull;
    z = (z ^ (z >> 30)) * 0xBF58476D1CE4E5B9ull; z = (z ^ (z >> 27)) * 0x94D049BB133111EBull; z ^= z >> 31;
    u32 zlo = ((u32)z & 0xFFFFF000u) | (((u32)E + 1u) & 0xFFFu), zhi = (u32)(z >> 32);
    asm volatile("" : "+s"(zlo), "+s"(zhi));
    const u32 ta = zlo & 0xFFFu, tb = ta ^ (0x800u | (zhi & 0x7FFu));
    asm volatile("" :: "v"(pf));
    if (X == 0) {
      if (lane == 0) *(LAS u32x4*)(L + L_TAG) = u32x4{ta, tb, zlo, zhi};
      LDS_WAIT(C_H, 4);
      f32x2 a = {0.f, 0.f};
#pragma unroll
      for (int w = 0; w < 4; ++w) { const LAS float* q = (const LAS float*)(L + L_QR + w * 512 + (lane & 31) * 16 + (lane >> 5) * 4); a[0] += q[0]; a[1] += q[2]; }
      __builtin_amdgcn_raw_buffer_store_b128(gran2<M28>(a[0], a[1], zlo, zhi), rs, WS_QG + ((qbr * 4 + (lane >> 5) * 2 + qkh) * 4096 + 512 * qh + 64 * qcb + 2 * (lane & 31)) * 8, 0, AUX_SC1);
    }
    {
      WAIT_UNTIL(t0 + QT, 2);
      const int qo = WS_QG + ((X * 4 + b * 2) * 4096 + 512 * h + 2 * lane) * 8;
      u32x4 gq[8];
      for (u32 spin = 0;; ++spin) {
        MEMFENCE();
#pragma unroll
        for (int i = 0; i < 8; ++i) gq[i] = __builtin_amdgcn_raw_buffer_load_b128(rs, qo + (i >> 1) * 1024, (i & 1) * 32768, AUX_SC1);
        u32 bad = 0;
#pragma unroll
        for (int i = 0; i < 8; ++i) bad |= gbad<M28>(gq[i], zlo, zhi);
        if (__builtin_amdgcn_ballot_w64(bad != 0) == 0 || spin >= SPIN_CAP) break;
        __builtin_amdgcn_s_sleep(2);
      }
#pragma unroll
      for (int i = 0; i < 4; ++i) { const f32x2 v = gval<M28>(gq[2 * i]) + gval<M28>(gq[2 * i + 1]); *(LAS u32*)(L + L_Q + (X * 4 + i) * QSTR + lane * 4) = cvt_pk(v[0], v[1]); }
      LDS_INC(C_Q);
    }
    int po = WS_R1 + ((wbr * 32 + whead) * 2 + X) * R1S + (lane >> 3) * 512 + wqd * 128 + (lane & 7) * 16;
    int pm = WS_ML + ((X * 8 + (whead >> 2)) * 16 + (lane >> 3) + 8 * (lane & 1)) * 128 + (wbr * 4 + (whead & 3)) * 16;
    int la = L_A + X * 64 + (lane & 7) * 8 + g * 2;
    asm volatile("" : "+v"(po), "+v"(pm), "+v"(la));
    if (X == 0) {
      LDS_WAIT(C_ML, 10);
      const bool on = lane < 32 || (lane & 7) < 2;
      f32x2 ml = *(const LAS f32x2*)(L + L_ML + (((lane >> 5) * 8 + (on ? lane & 7 : 0)) * 4 + ((lane >> 3) & 3)) * 8);
      if (!on) ml = f32x2{-INFINITY, 0.f};
      float Mb = fmaxf(ml[0], dpp<DPP_XOR1>(ml[0]));
      Mb = fmaxf(Mb, dpp<DPP_XOR2>(Mb)); Mb = fmaxf(Mb, dpp<DPP_HMIR>(Mb));
      const float fw = __builtin_amdgcn_exp2f((ml[0] - Mb) * LOG2E);
      float Lb = fw * ml[1];
      Lb += dpp<DPP_XOR1>(Lb); Lb += dpp<DPP_XOR2>(Lb); Lb += dpp<DPP_HMIR>(Lb);
      *(LAS float*)(L + L_FW + lane * 4) = fw;
      if ((lane & 7) == 0) __builtin_amdgcn_raw_buffer_store_b128(gran2<M24>(Mb, Lb, zlo, zhi), rs, WS_ML + ((b * 8 + h) * 16 + s) * 128 + lane * 2, 0, AUX_SC1);
      LDS_ADD(C_O, 257u);
    }
    WAIT_UNTIL(t0 + TEND, 4);
    float wt;
    u32 spin = 0;
    for (;; ++spin) {
      MEMFENCE();
      const u32x4 ml = __builtin_amdgcn_raw_buffer_load_b128(rs, pm, 0, AUX_SC1);
      if (__builtin_amdgcn_ballot_w64(gbad<M24>(ml, zlo, zhi) != 0) == 0 || spin >= SPIN_CAP) {
        const f32x2 m = gval<M24>(ml);
        float Mg = fmaxf(m[0], dpp<DPP_XOR1>(m[0]));
        Mg = xrow_max(fmaxf(Mg, dpp<DPP_ROR8>(Mg)));
        wt = __builtin_amdgcn_exp2f((m[0] - Mg) * LOG2E);
        float Lg = wt * m[1];
        Lg += dpp<DPP_XOR1>(Lg); Lg += dpp<DPP_ROR8>(Lg);
        wt *= 1.0f / xrow_sum(Lg);
        break;
      }
      __builtin_amdgcn_s_sleep(16);
    }
    float w0 = dpp<0xA0>(wt), w1 = dpp<0xF5>(wt);
    asm volatile("" : "+v"(w0), "+v"(w1));
    {
      u32 tp = t0 + TEND + OLATE;
      bool seen = false;
      for (;;) {
        const u32 now = now32();
        if ((int)(now - tp) >= 0) break;
        if (!seen && LDS_GET(C_O) >= O_ALL) { seen = true; if ((int)(now + ODT - tp) < 0) tp = now + ODT; }
        __builtin_amdgcn_s_sleep(1);
      }
    }
    u32x4 o0, o1;
    bool need = true;
    f32x4 a;
    for (;; ++spin) {
      MEMFENCE();
      if (need) { o0 = __builtin_amdgcn_raw_buffer_load_b128(rs, po, 0, AUX_SC1); o1 = __builtin_amdgcn_raw_buffer_load_b128(rs, po, 4096, AUX_SC1); }
      a = w0 * val4(o0) + w1 * val4(o1);
      asm volatile("" : "+v"(a));
      need = bad8(o0, o1, ta, tb);
      if (__builtin_amdgcn_ballot_w64(need) == 0 || spin >= SPIN_CAP) break;
    }
    const float ov = reduce8x4(a);
    *(LAS u16*)(L + la) = (u16)cvt_pk(ov, ov);
    BARRIER();
    return;
  }

  if (wave >= 12) {
    const int w = wave - 12;
    BARRIER();
    const rsrc_t rw = mk_rsrc(wbr ? woB : woA, 4096 * 1024);
    int a0 = (whead * 128 + 32 * wqd + 2 * g) * 1024 + w * 256 + c * 16;
    int a1 = WS_R2 + (wbr * 128 + whead * 4 + wqd) * R2S + w * 1024 + c * 64 + g * 16;
    int a2[4];
#pragma unroll
    for (int k = 0; k < 4; ++k) a2[k] = (c >> 1) == 2 * k ? L_A + ((c & 1) * 4 + g) * 16 : L_ZERO;
    asm volatile("" : "+v"(a0), "+v"(a1), "+v"(a2[0]), "+v"(a2[1]), "+v"(a2[2]), "+v"(a2[3]) : "v"(pf));
    WAIT_UNTIL(t0 + TEND - 80, 8);
    LDS_WAIT(C_ISS, 10);
    const u32x2 tg = *(const LAS u32x2*)(L + L_TAG);
    u32x4 Wo[8], Wt[8];
#pragma unroll
    for (int t = 0; t < 8; ++t) {
      WAIT_UNTIL(t0 + TEND + WABS + w * TAUW / 100 + t * 4 * TAUW / 100, 1);
      if (t == 4) LDS_WAIT(C_O, O_ALL);
      Wo[t] = __builtin_amdgcn_raw_buffer_load_b128(rw, a0 + (t >> 2) * 1024, (t & 3) * 8192, AUX_NT);
      SCHED();
    }
#pragma unroll
    for (int i = 0; i < 4; ++i) {
#pragma unroll
      for (int d = 0; d < 4; ++d) { Wt[2 * d][i] = pk_lo(Wo[2 * i][d], Wo[2 * i + 1][d]); Wt[2 * d + 1][i] = pk_hi(Wo[2 * i][d], Wo[2 * i + 1][d]); }
      if (i >= 2) {
#pragma unroll
        for (int e = 0; e < 8; ++e) asm volatile("" : "+v"(Wt[e]));
      }
      SCHED();
    }
    __builtin_amdgcn_s_setprio(3);
    BARRIER();
    u32x4 oa[4];
#pragma unroll
    for (int k = 0; k < 4; ++k) oa[k] = *(const LAS u32x4*)(L + a2[k]);
    f32x4 D0 = {0.f, 0.f, 0.f, 0.f}, D1 = D0;
#pragma unroll
    for (int k = 0; k < 4; ++k) { D0 = mfma32(oa[k], Wt[2 * k], D0); D1 = mfma32(oa[k], Wt[2 * k + 1], D1); }
    __builtin_amdgcn_raw_buffer_store_b128(pack4(f32x4{D0[0], D0[1], D1[0], D1[1]}, tg[0], tg[1]), rs, a1, 0, AUX_SC1);
    return;
  }

  u32x4 K[16], V[16];
  __builtin_amdgcn_s_setprio(3);
  u32x4 wq[8], xa;
  if (kw < 4) {
    const rsrc_t rwq = mk_rsrc(qbr ? wqB : wqA, 512 * 8192);
    const rsrc_t rx = mk_rsrc(qbr ? xB : xA, 2048);
#pragma unroll
    for (int t = 0; t < 8; ++t) wq[t] = __builtin_amdgcn_raw_buffer_load_b128(rwq, wqv, t * 8192, AUX_NT);
    xa = __builtin_amdgcn_raw_buffer_load_b128(rx, xv, 0, 0);
  }
  SCHED();
  __builtin_amdgcn_s_setprio(2);
  const rsrc_t rk = mk_rsrc(br ? kB : kA, 16 * SK * 256);
  const rsrc_t rv = mk_rsrc(br ? vB : vA, 16 * SK * 256);
#define LOADK(i) K[i] = __builtin_amdgcn_raw_buffer_load_b128(rk, kvo, toff[(i) >> 2] ^ (((i) & 3) * 1024), AUX_NT)
#define LOADV(i) V[i] = __builtin_amdgcn_raw_buffer_load_b128(rv, vvo, toff[(i) >> 2] ^ (((i) & 3) * 1024), AUX_NT)
#pragma unroll
  for (int i = 0; i < KPRE; ++i) {
    if (i == 1) BARRIER();
    LOADK(i);
    SCHED();
  }
  if (kw < 4) {
    u32x4 t[8];
    transpose8(wq, t);
    f32x4 D[8], r;
#pragma unroll
    for (int e = 0; e < 8; ++e) D[e] = mfma32(xa, t[e], f32x4{0.f, 0.f, 0.f, 0.f});
    float v[8][2];
#pragma unroll
    for (int e = 0; e < 8; ++e) { v[e][0] = D[e][0] + dpp<DPP_ROR8>(D[e][2]); v[e][1] = D[e][1] + dpp<DPP_ROR8>(D[e][3]); }
#pragma unroll
    for (int i = 0; i < 4; ++i) r[i] = merge_row<3>(merge_row<2>(merge_row<1>(v[i >> 1][i & 1], v[2 + (i >> 1)][i & 1]), v[4 + (i >> 1)][i & 1]), v[6 + (i >> 1)][i & 1]);
    if (c < 8) *(LAS f32x4*)(L + L_QR + kw * 512 + c * 64 + g * 16) = r;
    LDS_INC(C_H);
  }
  SCHED();
  __builtin_amdgcn_s_setprio(1);
  asm volatile("" :: "s"(t64));
  const u32 pace0 = t0 + PT0 + kw * TAU / 100;
#define PACE(n) do { asm volatile("s_waitcnt vmcnt(%0)" :: "n"(WIN - 1) : "memory"); WAIT_UNTIL(pace0 + ((n) - KPRE) * TAU / 10, 1); } while (0)
#pragma unroll
  for (int i = KPRE; i < 16; ++i) { PACE(i); LOADK(i); SCHED(); }
  asm volatile("" :: "v"(pf));
  __builtin_amdgcn_s_setprio(0);
#pragma unroll
  for (int i = 0; i < V1N; ++i) { if (i < V1N - VBURST) PACE(16 + i); LOADV(i); SCHED(); }
  LDS_WAIT(C_Q, 2);
  const u32x4 qf = *(LAS u32x4*)(L + L_Q + (br * 4 + (c & 3)) * QSTR + (4 * g + (c >> 2)) * 16);
  const u32x2 tg = *(const LAS u32x2*)(L + L_TAG);
  float sc[4];
#pragma unroll
  for (int i = 0; i < 4; ++i) {
    const f32x4 z4 = {0.f, 0.f, 0.f, 0.f};
    const f32x4 d0 = mfma32(K[4 * i], qf, z4), d1 = mfma32(K[4 * i + 1], qf, z4), d2 = mfma32(K[4 * i + 2], qf, z4), d3 = mfma32(K[4 * i + 3], qf, z4);
    const float v0 = ((d0[0] + from_bank<1>(d0[1])) + from_bank<2>(d0[2])) + from_bank<3>(d0[3]);
    const float v1 = ((d1[1] + from_bank<3>(d1[0])) + from_bank<1>(d1[2])) + from_bank<2>(d1[3]);
    const float v2 = ((d2[2] + from_bank<2>(d2[0])) + from_bank<3>(d2[1])) + from_bank<1>(d2[3]);
    const float v3 = ((d3[3] + from_bank<1>(d3[0])) + from_bank<2>(d3[1])) + from_bank<3>(d3[2]);
    sc[i] = merge_bank<3>(merge_bank<2>(merge_bank<1>(v0, v1), v2), v3);
  }
#pragma unroll
  for (int t = 0; t < 2; ++t) {
    const u32 r1 = cvt_pk(sc[2 * t], sc[2 * t + 1]);
    const u32 r2 = cvt_pk(bf_lo(r1) * SCALE, bf_hi(r1) * SCALE);
    sc[2 * t] = bf_lo(r2); sc[2 * t + 1] = bf_hi(r2);
  }
  float mx = fmaxf(fmaxf(sc[0], sc[1]), fmaxf(sc[2], sc[3]));
  mx = fmaxf(mx, dpp<DPP_ROR4>(mx)); mx = fmaxf(mx, dpp<DPP_ROR8>(mx));
  mx = xrow_max(mx);
  float lsum = 0.f;
  u32x4 P[2][4];
#pragma unroll
  for (int i = 0; i < 4; ++i) {
    const float pe = __builtin_amdgcn_exp2f((sc[i] - mx) * LOG2E);
    lsum += pe;
    const u32 w01 = cvt_pk(pe, from_bank<1>(pe));
    P[i >> 1][0][(i & 1) * 2] = to_bank<0, 0>(w01); P[i >> 1][0][(i & 1) * 2 + 1] = to_bank<2, 0>(w01);
    P[i >> 1][1][(i & 1) * 2] = to_bank<0, 1>(w01); P[i >> 1][1][(i & 1) * 2 + 1] = to_bank<2, 1>(w01);
    P[i >> 1][2][(i & 1) * 2] = to_bank<0, 2>(w01); P[i >> 1][2][(i & 1) * 2 + 1] = to_bank<2, 2>(w01);
    P[i >> 1][3][(i & 1) * 2] = to_bank<0, 3>(w01); P[i >> 1][3][(i & 1) * 2 + 1] = to_bank<2, 3>(w01);
  }
  lsum += dpp<DPP_ROR4>(lsum); lsum += dpp<DPP_ROR8>(lsum);
  lsum = xrow_sum(lsum);
  if (lane < 4) *(LAS f32x2*)(L + L_ML + (kw * 4 + lane) * 8) = f32x2{mx, lsum};
  LDS_INC(C_ML);
  SCHED();
#pragma unroll
  for (int i = V1N; i < 12; ++i) { PACE(16 + i); LOADV(i); SCHED(); }
  f32x4 O0 = {0.f, 0.f, 0.f, 0.f}, O1 = O0;
#pragma unroll
  for (int d = 0; d < 4; ++d) {
    u32x4 alo, ahi;
#pragma unroll
    for (int t = 0; t < 4; ++t) { alo[t] = pk_lo(V[2 * t][d], V[2 * t + 1][d]); ahi[t] = pk_hi(V[2 * t][d], V[2 * t + 1][d]); }
    O0 = mfma32(alo, P[0][d], O0);
    O1 = mfma32(ahi, P[0][d], O1);
  }
  SCHED();
  PACE(28); LOADV(12); SCHED();
  const int mbr = (kw >> 1) & 1, mh = (kw & 1) * 2 + (lane >> 5);
  int reco = WS_R1 + ((mbr * 32 + 4 * h + mh) * 2 + b) * R1S + s * 512 + (lane & 31) * 16;
  int opo = L_O + mbr * 8 * OW + (mh * 132 + (lane & 31) * 4) * 4, fo = L_FW + (mbr * 4 + mh) * 32;
  int oo_l = L_O + kw * OW + ((c & 3) * 132 + 32 * g + 8 * (c >> 2)) * 4;
  u32 oinc = br ? 256u : 1u;
  asm volatile("" : "+v"(reco), "+v"(opo), "+v"(fo), "+v"(oo_l), "+v"(oinc));
  SCHED();
#pragma unroll
  for (int i = 13; i < 16; ++i) { PACE(16 + i); LOADV(i); SCHED(); }
  LDS_INC(C_ISS);
  {
    u32x4 A[8];
#pragma unroll
    for (int t = 0; t < 3; ++t) {
#pragma unroll
      for (int d = 0; d < 4; ++d) { A[2 * d][t] = pk_lo(V[8 + 2 * t][d], V[9 + 2 * t][d]); A[2 * d + 1][t] = pk_hi(V[8 + 2 * t][d], V[9 + 2 * t][d]); }
      SCHED();
    }
#pragma unroll
    for (int e = 0; e < 8; ++e) asm volatile("" : "+v"(A[e]));
    SCHED();
#pragma unroll
    for (int d = 0; d < 4; ++d) {
      A[2 * d][3] = pk_lo(V[14][d], V[15][d]); A[2 * d + 1][3] = pk_hi(V[14][d], V[15][d]);
      if (d) { O0 = mfma32(A[2 * d - 2], P[1][d - 1], O0); O1 = mfma32(A[2 * d - 1], P[1][d - 1], O1); }
      SCHED();
    }
    O0 = mfma32(A[6], P[1][3], O0); O1 = mfma32(A[7], P[1][3], O1);
  }
  *(LAS f32x4*)(L + oo_l) = O0;
  *(LAS f32x4*)(L + oo_l + 16) = O1;
  LDS_ADD(C_O, oinc);
  if (kw >= 8) return;
  if (kw < 4) {
    f32x4 a;
    if (kw < 2) {
      f32x4 f, f2, v[8];
      for (u32 spin = 0; spin < (SPIN_CAP << 6); ++spin) {
        MEMFENCE();
        const u32 cnt = LDS_LOAD(C_O);
        MEMFENCE();
        f = *(const LAS f32x4*)(L + fo); f2 = *(const LAS f32x4*)(L + fo + 16);
#pragma unroll
        for (int i = 0; i < 8; ++i) v[i] = *(const LAS f32x4*)(L + opo + i * OW);
        SCHED();
        if (((u32)__builtin_amdgcn_readfirstlane(cnt) & 255u) >= 9) break;
      }
      a = ((f[0] * v[0] + f[1] * v[1]) + (f[2] * v[2] + f[3] * v[3])) + ((f2[0] * v[4] + f2[1] * v[5]) + (f2[2] * v[6] + f2[3] * v[7]));
    } else {
      f32x2 f; f32x4 v0, v1;
      for (u32 spin = 0; spin < (SPIN_CAP << 6); ++spin) {
        MEMFENCE();
        const u32 cnt = LDS_LOAD(C_O);
        MEMFENCE();
        f = *(const LAS f32x2*)(L + fo); v0 = *(const LAS f32x4*)(L + opo); v1 = *(const LAS f32x4*)(L + opo + OW);
        SCHED();
        if ((u32)__builtin_amdgcn_readfirstlane(cnt) >= 3 * 256) break;
      }
      a = f[0] * v0 + f[1] * v1;
    }
    __builtin_amdgcn_raw_buffer_store_b128(pack4(a, tg[0], tg[1]), rs, reco, 0, AUX_SC1);
  }
  if (j & 3) return;
  SCHED();
  const int rbr = 1 - wbr, rcg = (h & 3) * 8 + (j >> 2);
  int off2 = WS_R2 + (rbr * 128 + kw * 16 + (lane >> 3)) * R2S + rcg * 128 + (lane & 7) * 16;
  int lf = L_F + (((lane & 7) * 4 + g) * 8 + kw) * 4, lfr = L_F + (lane & 31) * 32;
  int oo = (lane < 32 && !(lane & 2)) ? ((lane & 1) * 1024 + rbr * 512 + 16 * rcg + 2 * (lane >> 2)) * 2 : 4096;
  const rsrc_t ro = mk_rsrc(outp, 4096);
  asm volatile("" : "+v"(off2), "+v"(lf), "+v"(lfr), "+v"(oo));
  BARRIER();
  __builtin_amdgcn_s_sleep(P2DELAY);
  {
    f32x4 a;
    for (u32 spin = 0;; ++spin) {
      MEMFENCE();
      const u32x4 r0 = __builtin_amdgcn_raw_buffer_load_b128(rs, off2, 0, AUX_SC1), r1 = __builtin_amdgcn_raw_buffer_load_b128(rs, off2, 8 * R2S, AUX_SC1);
      a = val4(r0) + val4(r1);
      asm volatile("" : "+v"(a));
      if (__builtin_amdgcn_ballot_w64(bad8(r0, r1, tg[0], tg[1])) == 0 || spin >= SPIN_CAP) break;
    }
    *(LAS float*)(L + lf) = reduce8x4(a);
  }
  BARRIER();
  if (kw == 0) {
    const f32x4 x = *(const LAS f32x4*)(L + lfr) + *(const LAS f32x4*)(L + lfr + 16);
    const float v = (x[0] + x[1]) + (x[2] + x[3]);
    __builtin_amdgcn_raw_buffer_store_b32(cvt_pk(v, dpp<DPP_XOR2>(v)), ro, oo, 0, 0);
    if (blk == 0 && lane == 0) __builtin_amdgcn_raw_buffer_store_b64(*(const LAS u32x2*)(L + L_TAG + 8), rs, WS_EPOCH, 0, AUX_SC1);
  }
}

extern "C" void kernel_launch(void* const* d_in, const int* in_sizes, int n_in, void* d_out, int out_size, void* d_ws, size_t ws_size, hipStream_t stream) {
  Params p;
  p.x[0] = (const u16*)d_in[0]; p.k[0] = (const u16*)d_in[1]; p.v[0] = (const u16*)d_in[2]; p.wq[0] = (const u16*)d_in[3]; p.wo[0] = (const u16*)d_in[4];
  p.x[1] = (const u16*)d_in[5]; p.k[1] = (const u16*)d_in[6]; p.v[1] = (const u16*)d_in[7]; p.wq[1] = (const u16*)d_in[8]; p.wo[1] = (const u16*)d_in[9];
  p.out = (u16*)d_out; p.ws = (unsigned char*)d_ws;
  fused_decode<<<dim3(256), dim3(NTHR), L_END, stream>>>(p);
}
```
